```python
import torch

def _softmax(x, axis=-1):
    return torch.softmax(x, dim=axis)

def _gelu(x):
    return torch.nn.functional.gelu(x, approximate='tanh')

def _silu(x):
    return torch.nn.functional.silu(x)

def _sigmoid(x):
    return torch.sigmoid(x)

def _layernorm(x, axis=-1, eps=1e-05):
    m = x.mean(dim=axis, keepdim=True)
    v = x.var(dim=axis, keepdim=True, unbiased=False)
    return (x - m) / torch.sqrt(v + eps)

def _scatter_add(tgt, idx, src, axis=0):
    return tgt.clone().index_add_(axis, idx, src)

def _segment_sum(src, seg, n):
    out = torch.zeros((n,) + src.shape[1:], dtype=src.dtype)
    return out.index_add_(0, seg, src)

def _pad(x, pw):
    flat = tuple((v for pair in reversed(pw) for v in pair))
    return torch.nn.functional.pad(x, flat)
kernel_name = 'adaLN_cond__memory_aug_recurrent_mem_tr__glu_gated_swiglu__prompt'

def setup_inputs():

    _w = lambda a: (lambda t: t.to(torch.bfloat16) if t.is_floating_point() else t)(torch.as_tensor(a))

    def _A_setup():
        B, S, Dm = (1, 2048, 640)
        H, Dh = (32, 128)
        M, seg = (8, 32)
        seg = min(seg, S)
        S = S // seg * seg
        return {'x': _w((lambda *s: torch.randn(*s, dtype=torch.bfloat16) * 0.05)(B, S, Dm)), 'mem0': _w((lambda *s: torch.randn(*s, dtype=torch.bfloat16) * 0.8)(B, M, Dm)), 'Wq': _w((lambda *s: torch.randn(*s, dtype=torch.bfloat16) * 0.8)(Dm, H, Dh) * Dm ** (-0.5)), 'Wk': _w((lambda *s: torch.randn(*s, dtype=torch.bfloat16) * 0.8)(Dm, H, Dh) * Dm ** (-0.5)), 'Wv': _w((lambda *s: torch.randn(*s, dtype=torch.bfloat16) * 0.8)(Dm, H, Dh) * Dm ** (-0.5)), 'Wo': _w((lambda *s: torch.randn(*s, dtype=torch.bfloat16) * 0.8)(H, Dh, Dm) * (H * Dh) ** (-0.5)), 'seg_len': seg}
    _A_in = _A_setup()

    def _B_setup():
        B, S, D, F = (1, 2048, 640, 1728)
        return {'x': _w((lambda *s: torch.randn(*s, dtype=torch.bfloat16) * 0.05)(B, S, D)), 'w1': _w((lambda *s: torch.randn(*s, dtype=torch.bfloat16) * 0.4)(D, F) * (2.0 / D) ** 0.5), 'w3': _w((lambda *s: torch.randn(*s, dtype=torch.bfloat16) * 0.4)(D, F) * (2.0 / D) ** 0.5), 'w2': _w((lambda *s: torch.randn(*s, dtype=torch.bfloat16) * 0.4)(F, D) * (2.0 / F) ** 0.5)}
    _B_in = _B_setup()
    return {'A_x': _A_in['x'], 'A_mem0': _A_in['mem0'], 'A_Wq': _A_in['Wq'], 'A_Wk': _A_in['Wk'], 'A_Wv': _A_in['Wv'], 'A_Wo': _A_in['Wo'], 'A_seg_len': _A_in['seg_len'], 'B_x': _B_in['x'], 'B_w1': _B_in['w1'], 'B_w3': _B_in['w3'], 'B_w2': _B_in['w2'], 'c': _w((lambda *s: torch.randn(*s, dtype=torch.bfloat16) * 0.1)(1, 640) * 8.0), 'w_mod': _w((lambda *s: torch.randn(*s, dtype=torch.bfloat16) * 0.1)(640, 6 * 640) * 640 ** (-0.5) * 8.0)}

def reference(A_x, A_mem0, A_Wq, A_Wk, A_Wv, A_Wo, A_seg_len, B_x, B_w1, B_w3, B_w2, c, w_mod):

    def _A_ref(x, mem0, Wq, Wk, Wv, Wo, seg_len):
        B, S, Dm = x.shape
        H, Dh = (Wq.shape[1], Wq.shape[2])
        M = mem0.shape[1]
        scale = Dh ** (-0.5)
        n_seg = S // seg_len
        L = M + seg_len
        idx = torch.arange(L)
        mask = (idx[:, None] < M) | (idx[:, None] >= idx[None, :])
        mem = mem0
        outs = []
        for s in range(n_seg):
            xs = x[:, s * seg_len:(s + 1) * seg_len, :].clone()
            h = torch.concatenate([mem, xs], axis=1)
            q = torch.einsum('btd,dhe->bhte', h, Wq)
            k = torch.einsum('btd,dhe->bhte', h, Wk)
            v = torch.einsum('btd,dhe->bhte', h, Wv)
            scores = torch.einsum('bhqe,bhke->bhqk', q, k) * scale
            scores = torch.where(mask[None, None], scores, -1000000000.0)
            p = _softmax(scores, axis=-1)
            o = torch.einsum('bhqk,bhke->bhqe', p, v)
            o = torch.einsum('bhte,hed->btd', o, Wo)
            mem = o[:, :M, :].clone()
            outs.append(o[:, M:, :])
        return torch.concatenate(outs, axis=1)

    def _B_ref(x, w1, w3, w2):
        gate = torch.einsum('bsd,df->bsf', x, w1)
        up = torch.einsum('bsd,df->bsf', x, w3)
        h = _silu(gate) * up
        out = torch.einsum('bsf,fd->bsd', h, w2)
        return out
    x = A_x
    mod = (_silu(c) @ w_mod).reshape(c.shape[0], 6, 1, -1)
    sa, ca, ga, sb, cb, gb = (mod[:, 0].clone(), mod[:, 1].clone(), mod[:, 2].clone(), mod[:, 3].clone(), mod[:, 4].clone(), mod[:, 5].clone())
    h = _layernorm(x, axis=-1) * (1 + ca) + sa
    x = x + ga * _A_ref(h, A_mem0, A_Wq, A_Wk, A_Wv, A_Wo, A_seg_len)
    h = _layernorm(x, axis=-1) * (1 + cb) + sb
    x = x + gb * _B_ref(h, B_w1, B_w3, B_w2)
    return x
```

```cpp
#include <hip/hip_runtime.h>
#include <hip/hip_bf16.h>
typedef __hip_bfloat16 bf;
__device__ __forceinline__ float b2f(bf v) { return __bfloat162float(v); }
__device__ __forceinline__ bf f2b(float v) { return __float2bfloat16(v); }
__device__ __forceinline__ float rb(float v) { return b2f(f2b(v)); }
constexpr int S = 2048, D = 640, H = 32, E = 128, M = 8, SEG = 32, F = 1728, L = 40;

__global__ void k_mod(const bf* c, const bf* w, bf* mod) {
  int n = blockIdx.x * blockDim.x + threadIdx.x; if (n >= 6 * D) return;
  float acc = 0;
  for (int d = 0; d < D; ++d) { float cv = b2f(c[d]); float s = rb(cv / (1.f + expf(-cv))); acc += s * b2f(w[(size_t)d * 6 * D + n]); }
  mod[n] = f2b(acc);
}
__global__ void k_ln(const bf* x, const bf* shift, const bf* scale, bf* h) {
  int r = blockIdx.x, t = threadIdx.x; float v[10]; float s = 0;
  for (int i = 0; i < 10; ++i) { v[i] = b2f(x[(size_t)r * D + t + 64 * i]); s += v[i]; }
  for (int o = 32; o; o >>= 1) s += __shfl_xor(s, o);
  float m = s / D; float q = 0;
  for (int i = 0; i < 10; ++i) q += (v[i] - m) * (v[i] - m);
  for (int o = 32; o; o >>= 1) q += __shfl_xor(q, o);
  float rs = rsqrtf(q / D + 1e-5f);
  for (int i = 0; i < 10; ++i) { int c = t + 64 * i; h[(size_t)r * D + c] = f2b((v[i] - m) * rs * (1.f + b2f(scale[c])) + b2f(shift[c])); }
}
__global__ void k_gemm(const bf* A, int lda, const bf* B, int ldb, bf* C, int ldc, int Mr, int N, int K) {
  __shared__ float As[16][17], Bs[16][17];
  int tx = threadIdx.x, ty = threadIdx.y; int r = blockIdx.y * 16 + ty, c = blockIdx.x * 16 + tx; float acc = 0;
  for (int k0 = 0; k0 < K; k0 += 16) {
    As[ty][tx] = r < Mr ? b2f(A[(size_t)r * lda + k0 + tx]) : 0.f;
    Bs[ty][tx] = b2f(B[(size_t)(k0 + ty) * ldb + c]);
    __syncthreads();
    for (int k = 0; k < 16; ++k) acc += As[ty][k] * Bs[k][tx];
    __syncthreads();
  }
  if (r < Mr) C[(size_t)r * ldc + c] = f2b(acc);
}
__global__ void __launch_bounds__(128) k_attn(const bf* qm, const bf* km, const bf* vm, const bf* qx, const bf* kx, const bf* vx, bf* om, bf* ox) {
  __shared__ float sc[L][L];
  int h = blockIdx.x, t = threadIdx.x;
  for (int p = t; p < L * L; p += 128) { int i = p / L, j = p % L;
    const bf* q = i < M ? qm + i * 4096 + h * E : qx + (i - M) * 4096 + h * E;
    const bf* k = j < M ? km + j * 4096 + h * E : kx + (j - M) * 4096 + h * E;
    float a = 0; for (int e = 0; e < E; ++e) a += b2f(q[e]) * b2f(k[e]);
    a *= 0.08838834764831845f;
    bool ok = (i < M) || (i >= j);
    sc[i][j] = ok ? a : -1e9f; }
  __syncthreads();
  if (t < L) { float m = -3e38f; for (int j = 0; j < L; ++j) m = fmaxf(m, sc[t][j]); float s = 0; for (int j = 0; j < L; ++j) { float e = __expf(sc[t][j] - m); sc[t][j] = e; s += e; }
    for (int j = 0; j < L; ++j) sc[t][j] = rb(sc[t][j] / s); }
  __syncthreads();
  for (int i = 0; i < L; ++i) { float a = 0; for (int j = 0; j < L; ++j) { const bf* v = j < M ? vm + j * 4096 + h * E : vx + (j - M) * 4096 + h * E; a += sc[i][j] * b2f(v[t]); }
    if (i < M) om[i * 4096 + h * E + t] = f2b(a); else ox[(i - M) * 4096 + h * E + t] = f2b(a); }
}
__global__ void k_res(const bf* x, const bf* g, const bf* y, bf* o) { size_t i = blockIdx.x * (size_t)blockDim.x + threadIdx.x; int c = i % D; o[i] = f2b(b2f(x[i]) + rb(b2f(g[c]) * b2f(y[i]))); }
__global__ void k_swi(const bf* g, const bf* u, bf* o) { size_t i = blockIdx.x * (size_t)blockDim.x + threadIdx.x; float a = b2f(g[i]); o[i] = f2b(rb(a / (1.f + __expf(-a))) * b2f(u[i])); }

extern "C" void kernel_launch(void* const* d_in, const int* in_sizes, int n_in, void* d_out, int out_size, void* d_ws, size_t ws_size, hipStream_t st) {
  const bf* x = (const bf*)d_in[0]; const bf* mem0 = (const bf*)d_in[1]; const bf* Wq = (const bf*)d_in[2]; const bf* Wk = (const bf*)d_in[3]; const bf* Wv = (const bf*)d_in[4]; const bf* Wo = (const bf*)d_in[5];
  const bf* w1 = (const bf*)d_in[8]; const bf* w3 = (const bf*)d_in[9]; const bf* w2 = (const bf*)d_in[10]; const bf* c = (const bf*)d_in[11]; const bf* wm = (const bf*)d_in[12];
  bf* ws = (bf*)d_ws; size_t o = 0;
  auto al = [&](size_t n) { bf* p = ws + o; o += (n + 127) / 128 * 128; return p; };
  bf* mod = al(3840); bf* h = al((size_t)S * D); bf* Q = al((size_t)S * 4096); bf* K = al((size_t)S * 4096); bf* V = al((size_t)S * 4096); bf* O = al((size_t)S * 4096);
  bf* qm = al(8 * 4096); bf* km = al(8 * 4096); bf* vm = al(8 * 4096); bf* om = al(8 * 4096); bf* mem = al(8 * D);
  bf* att = al((size_t)S * D); bf* x1 = al((size_t)S * D); bf* g = al((size_t)S * F); bf* u = al((size_t)S * F); bf* hf = al((size_t)S * F); bf* y = al((size_t)S * D);
  k_mod<<<15, 256, 0, st>>>(c, wm, mod);
  k_ln<<<S, 64, 0, st>>>(x, mod, mod + D, h);
  dim3 b(16, 16);
  k_gemm<<<dim3(256, 128), b, 0, st>>>(h, D, Wq, 4096, Q, 4096, S, 4096, D);
  k_gemm<<<dim3(256, 128), b, 0, st>>>(h, D, Wk, 4096, K, 4096, S, 4096, D);
  k_gemm<<<dim3(256, 128), b, 0, st>>>(h, D, Wv, 4096, V, 4096, S, 4096, D);
  for (int s = 0; s < 64; ++s) {
    const bf* m = s ? mem : mem0;
    k_gemm<<<dim3(256, 1), b, 0, st>>>(m, D, Wq, 4096, qm, 4096, 8, 4096, D);
    k_gemm<<<dim3(256, 1), b, 0, st>>>(m, D, Wk, 4096, km, 4096, 8, 4096, D);
    k_gemm<<<dim3(256, 1), b, 0, st>>>(m, D, Wv, 4096, vm, 4096, 8, 4096, D);
    size_t so = (size_t)s * 32 * 4096;
    k_attn<<<32, 128, 0, st>>>(qm, km, vm, Q + so, K + so, V + so, om, O + so);
    k_gemm<<<dim3(40, 1), b, 0, st>>>(om, 4096, Wo, D, mem, D, 8, D, 4096);
  }
  k_gemm<<<dim3(40, 128), b, 0, st>>>(O, 4096, Wo, D, att, D, S, D, 4096);
  k_res<<<S * D / 256, 256, 0, st>>>(x, mod + 2 * D, att, x1);
  k_ln<<<S, 64, 0, st>>>(x1, mod + 3 * D, mod + 4 * D, h);
  k_gemm<<<dim3(108, 128), b, 0, st>>>(h, D, w1, F, g, F, S, F, D);
  k_gemm<<<dim3(108, 128), b, 0, st>>>(h, D, w3, F, u, F, S, F, D);
  k_swi<<<S * F / 256, 256, 0, st>>>(g, u, hf);
  k_gemm<<<dim3(40, 128), b, 0, st>>>(hf, F, w2, D, y, D, S, D, F);
  k_res<<<S * D / 256, 256, 0, st>>>(x1, mod + 5 * D, y, (bf*)d_out);
}
```

```cpp
#include <hip/hip_runtime.h>
#include <hip/hip_bf16.h>
#include <stdint.h>

namespace {

typedef __bf16 bf16x2 __attribute__((ext_vector_type(2)));
typedef __bf16 bf16x4 __attribute__((ext_vector_type(4)));
typedef __bf16 bf16x8 __attribute__((ext_vector_type(8)));
typedef __bf16 bf16x16 __attribute__((ext_vector_type(16)));
typedef short s16x4 __attribute__((ext_vector_type(4)));
typedef short s16x8 __attribute__((ext_vector_type(8)));
typedef float f32x4 __attribute__((ext_vector_type(4)));
typedef float f32x2 __attribute__((ext_vector_type(2)));
typedef unsigned v4u __attribute__((ext_vector_type(4)));
typedef unsigned v2u __attribute__((ext_vector_type(2)));
typedef unsigned long long u64;
typedef unsigned short u16;
#define LAS __attribute__((address_space(3)))
#define DEV __device__ __forceinline__

constexpr int NSEG = 64, DM = 640, NH = 32, FF = 1728;
constexpr int QW = 3;

constexpr unsigned CTRL_BYTES = 69632;
constexpr unsigned TICK_OFF = 0;
constexpr unsigned PHASE_OFF = 1024;
constexpr unsigned PG_QK = 2048;
constexpr unsigned PG_VU = PG_QK + 32 * 128;
constexpr unsigned PG_KM = PG_VU + 32 * 128;
constexpr unsigned PG_Z = PG_KM + 32 * 128;
constexpr unsigned PG_P = PG_Z + 80 * 128;
constexpr unsigned PG_X1 = PG_P + 64 * 128;
constexpr unsigned PG_HF = PG_X1 + 20 * 128;
constexpr unsigned PG_B = PG_HF + 27 * 128;
constexpr unsigned PG_H = PG_B + 160 * 128;
static_assert(PG_H + 64 * 128 <= CTRL_BYTES, "ctrl");
constexpr unsigned MOD_OFF = CTRL_BYTES;
constexpr unsigned RING_MIN = 131072;
constexpr unsigned RING_BYTES = 458752;
constexpr unsigned RP_L = 0, RP_STR = 49152;
constexpr unsigned RMS_L = 98304, RMC_L = 163840, RM_STR = 32768;
constexpr unsigned KX_OFF = 2u << 20;
constexpr unsigned UC_OFF = KX_OFF + 64u * 32 * 8192;
constexpr unsigned WC_OFF = UC_OFF + 64u * 80 * 16384;
constexpr unsigned KM_OFF = WC_OFF + 80u * 327680;
constexpr unsigned ZC_OFF = KM_OFF + 64u * 32 * 2048;
constexpr unsigned PT_OFF = ZC_OFF + 64u * 80 * 4096;
constexpr unsigned PM_OFF = PT_OFF + 64u * 2 * 32 * 1024;
constexpr unsigned X1_OFF = PM_OFF + 64u * 2 * 8 * 1024;
constexpr unsigned HF_OFF = X1_OFF + 2048u * 1280;
constexpr unsigned HI_OFF = HF_OFF + 64u * 2 * 54 * 1024;
constexpr unsigned WS_END = HI_OFF + 64u * 40960;
static_assert(WS_END <= (256u << 20), "workspace");

constexpr int LDS_BYTES = 114688;
constexpr unsigned SPIN_BUDGET = 1u << 21;

struct Params {
  const u16* x; const u16* mem0; const u16* Wq; const u16* Wk; const u16* Wv; const u16* Wo;
  const u16* w1; const u16* w3; const u16* w2; const u16* c; const u16* wmod;
  u16* out; char* ws;
};

DEV unsigned pk2(float a, float b) { bf16x2 v = {(__bf16)a, (__bf16)b}; return __builtin_bit_cast(unsigned, v); }
DEV float bflo(unsigned u) { return __uint_as_float(u << 16); }
DEV float bfhi(unsigned u) { return __uint_as_float(u & 0xffff0000u); }
DEV float rbf(float a) { return (float)(__bf16)a; }
DEV unsigned nosent(unsigned u) { return u < 0xfffffffeu ? u : 0xfffffffeu; }
DEV void wg_barrier() { asm volatile("s_waitcnt lgkmcnt(0)\n\ts_barrier" ::: "memory"); }
DEV void drain_vm() { asm volatile("s_waitcnt vmcnt(0)" ::: "memory"); }
DEV void wait_lds() { asm volatile("s_waitcnt lgkmcnt(0)" ::: "memory"); }
DEV unsigned remap(unsigned l, unsigned phase) { return ((l >> 13) << 14) | (phase << 13) | (l & 8191u); }
DEV unsigned ring_off(const char* ws) {
  const u64 a = (u64)ws;
  return (unsigned)(((a + RING_MIN + 524287ull) & ~524287ull) - a);
}
typedef __amdgpu_buffer_rsrc_t rsrc_t;
DEV v4u ldw16(rsrc_t r, unsigned vo, unsigned so) { return __builtin_amdgcn_raw_buffer_load_b128(r, vo, so, 16); }
DEV v2u ldw8(rsrc_t r, unsigned vo, unsigned so) { return __builtin_amdgcn_raw_buffer_load_b64(r, vo, so, 16); }
DEV unsigned ldw4(rsrc_t r, unsigned vo, unsigned so) { return __builtin_amdgcn_raw_buffer_load_b32(r, vo, so, 16); }
DEV void stw16(rsrc_t r, unsigned vo, unsigned so, v4u v) { __builtin_amdgcn_raw_buffer_store_b128(v, r, vo, so, 16); }
DEV void stw8(rsrc_t r, unsigned vo, unsigned so, v2u v) { __builtin_amdgcn_raw_buffer_store_b64(v, r, vo, so, 16); }
DEV void stw4(rsrc_t r, unsigned vo, unsigned so, unsigned v) { __builtin_amdgcn_raw_buffer_store_b32(v, r, vo, so, 16); }
DEV v4u lds16(LAS char* L, unsigned o) { return *(LAS v4u*)(L + o); }
DEV void sts16(LAS char* L, unsigned o, v4u v) { *(LAS v4u*)(L + o) = v; }
DEV void sts8(LAS char* L, unsigned o, v2u v) { *(LAS v2u*)(L + o) = v; }
DEV v2u ldtr(LAS char* L, unsigned o) {
  return __builtin_bit_cast(v2u, __builtin_amdgcn_ds_read_tr16_b64_v4bf16((LAS bf16x4*)(L + o)));
}
DEV v4u cat(v2u a, v2u b) { v4u r = {a.x, a.y, b.x, b.y}; return r; }
DEV f32x4 mfma(v4u a, v4u b, f32x4 c) {
  return __builtin_amdgcn_mfma_f32_16x16x32_bf16(__builtin_bit_cast(bf16x8, a), __builtin_bit_cast(bf16x8, b), c, 0, 0, 0);
}
DEV f32x4 smf64(v4u a, v4u b0, v4u b1, f32x4 c, int idx) {
  bf16x16 b = __builtin_shufflevector(__builtin_bit_cast(bf16x8, b0), __builtin_bit_cast(bf16x8, b1), 0, 1, 2, 3, 4, 5, 6, 7, 8, 9, 10,
                                      11, 12, 13, 14, 15);
  return __builtin_amdgcn_smfmac_f32_16x16x64_bf16(__builtin_bit_cast(bf16x8, a), b, c, idx, 0, 0);
}
DEV f32x4 smf32(v2u a, v4u b, f32x4 c, int idx) {
  return __builtin_amdgcn_smfmac_f32_16x16x32_bf16(__builtin_bit_cast(s16x4, a), __builtin_bit_cast(s16x8, b), c, idx, 0, 0);
}
template <int CTRL> DEV float dppf(float v) {
  return __int_as_float(__builtin_amdgcn_update_dpp(0, __float_as_int(v), CTRL, 0xf, 0xf, true));
}
DEV float sum16(float v) {
  v += dppf<0xb1>(v); v += dppf<0x4e>(v); v += dppf<0x141>(v); v += dppf<0x140>(v);
  return v;
}
DEV float max_x16(float v) { unsigned u = __float_as_uint(v); auto r = __builtin_amdgcn_permlane16_swap(u, u, false, false); unsigned a = r[0], b = r[1]; return fmaxf(__uint_as_float(a), __uint_as_float(b)); }
DEV float max_x32(float v) { unsigned u = __float_as_uint(v); auto r = __builtin_amdgcn_permlane32_swap(u, u, false, false); unsigned a = r[0], b = r[1]; return fmaxf(__uint_as_float(a), __uint_as_float(b)); }
DEV float add_x16(float v) { unsigned u = __float_as_uint(v); auto r = __builtin_amdgcn_permlane16_swap(u, u, false, false); unsigned a = r[0], b = r[1]; return __uint_as_float(a) + __uint_as_float(b); }
DEV float add_x32(float v) { unsigned u = __float_as_uint(v); auto r = __builtin_amdgcn_permlane32_swap(u, u, false, false); unsigned a = r[0], b = r[1]; return __uint_as_float(a) + __uint_as_float(b); }
DEV float swapadd32(float a, float b) {
  auto r = __builtin_amdgcn_permlane32_swap(__float_as_uint(a), __float_as_uint(b), false, false);
  unsigned x = r[0], y = r[1];
  return __uint_as_float(x) + __uint_as_float(y);
}

struct Ctx {
  rsrc_t ws;
  v4u srd;
  LAS char* L;
  int tid, lane, wave;
  unsigned budget;
};

DEV void wait_prog(Ctx& c, unsigned off, int n, int need) {
  const unsigned vo = off + (unsigned)(c.lane < n ? c.lane : 0) * 128u;
  for (;;) {
    asm volatile("" ::: "memory");
    const int v = (int)ldw4(c.ws, vo, 0);
    if (__all(v >= need)) break;
    if (c.budget == 0) break;
    --c.budget;
    __builtin_amdgcn_s_sleep(2);
  }
  asm volatile("" ::: "memory");
}
DEV void set_prog(Ctx& c, unsigned off, int v) { stw4(c.ws, off, 0, (unsigned)v); }

#define P_LDA "buffer_load_dwordx4 v[240:243], %[va], %[srd], %[so] offen sc1\n\t"
#define P_LDB "buffer_load_dwordx4 v[244:247], %[vb], %[srd], %[so] offen sc1\n\t"
#define P_LDC "buffer_load_dwordx2 v[248:249], %[vc], %[srd], %[so] offen sc1\n\t"
#define P_CKA "v_max3_u32 v250, v240, v241, v242\n\tv_max_u32 v250, v250, v243\n\tv_cmp_eq_u32 vcc, -1, v250\n\t"
#define P_CKB "v_max3_u32 v250, v244, v245, v246\n\tv_max_u32 v250, v250, v247\n\tv_cmp_eq_u32 vcc, -1, v250\n\t"
#define P_CKC "v_max_u32 v250, v248, v249\n\tv_cmp_eq_u32 vcc, -1, v250\n\t"
#define P_BUD "s_sub_u32 %[cnt], %[cnt], 1\n\ts_cbranch_scc1 98f\n\t"
#define P_END "s_branch 99f\n\t98:\n\ts_mov_b32 %[cnt], 0\n\t99:\n\ts_waitcnt vmcnt(0)\n\t"
DEV void poll2(Ctx& c, unsigned va, unsigned vb, unsigned so, v4u& a, v4u& b) {
  unsigned cnt = __builtin_amdgcn_readfirstlane(c.budget);
  asm volatile(
      "s_waitcnt vmcnt(0)\n\ts_nop 4\n\t" P_LDA P_LDB
      "10:\n\t"
      "s_waitcnt vmcnt(1)\n\t" P_CKA "s_cbranch_vccz 21f\n\t" P_LDA
      "s_waitcnt vmcnt(1)\n\t" P_CKB "s_cbranch_vccz 20f\n\t" P_LDB P_BUD "s_branch 10b\n\t"
      "20:\n\t"
      "s_waitcnt vmcnt(0)\n\t" P_CKA "s_cbranch_vccz 99f\n\t" P_LDA P_BUD "s_branch 20b\n\t"
      "21:\n\t"
      "s_waitcnt vmcnt(0)\n\t" P_CKB "s_cbranch_vccz 99f\n\t" P_LDB P_BUD "s_branch 21b\n\t" P_END
      : "=&{v[240:243]}"(a), "=&{v[244:247]}"(b), [cnt] "+s"(cnt)
      : [va] "v"(va), [vb] "v"(vb), [srd] "s"(c.srd), [so] "s"(so)
      : "v250", "vcc", "scc", "memory");
  c.budget = __builtin_amdgcn_readfirstlane(cnt);
}
DEV void poll3(Ctx& c, unsigned va, unsigned vb, unsigned vc, unsigned so, v4u& a, v4u& b, v2u& d) {
  unsigned cnt = __builtin_amdgcn_readfirstlane(c.budget);
  asm volatile(
      "s_waitcnt vmcnt(0)\n\ts_nop 4\n\t" P_LDA P_LDB P_LDC
      "10:\n\t"
      "s_waitcnt vmcnt(2)\n\t" P_CKA "s_cbranch_vccz 31f\n\t" P_LDA
      "s_waitcnt vmcnt(2)\n\t" P_CKB "s_cbranch_vccz 32f\n\t" P_LDB
      "s_waitcnt vmcnt(2)\n\t" P_CKC "s_cbranch_vccz 33f\n\t" P_LDC P_BUD "s_branch 10b\n\t"
      "31:\n\t"
      "s_waitcnt vmcnt(1)\n\t" P_CKB "s_cbranch_vccz 22f\n\t" P_LDB
      "s_waitcnt vmcnt(1)\n\t" P_CKC "s_cbranch_vccz 21f\n\t" P_LDC P_BUD "s_branch 31b\n\t"
      "32:\n\t"
      "s_waitcnt vmcnt(1)\n\t" P_CKC "s_cbranch_vccz 20f\n\t" P_LDC
      "s_waitcnt vmcnt(1)\n\t" P_CKA "s_cbranch_vccz 22f\n\t" P_LDA P_BUD "s_branch 32b\n\t"
      "33:\n\t"
      "s_waitcnt vmcnt(1)\n\t" P_CKA "s_cbranch_vccz 21f\n\t" P_LDA
      "s_waitcnt vmcnt(1)\n\t" P_CKB "s_cbranch_vccz 20f\n\t" P_LDB P_BUD "s_branch 33b\n\t"
      "20:\n\t"
      "s_waitcnt vmcnt(0)\n\t" P_CKA "s_cbranch_vccz 99f\n\t" P_LDA P_BUD "s_branch 20b\n\t"
      "21:\n\t"
      "s_waitcnt vmcnt(0)\n\t" P_CKB "s_cbranch_vccz 99f\n\t" P_LDB P_BUD "s_branch 21b\n\t"
      "22:\n\t"
      "s_waitcnt vmcnt(0)\n\t" P_CKC "s_cbranch_vccz 99f\n\t" P_LDC P_BUD "s_branch 22b\n\t" P_END
      : "=&{v[240:243]}"(a), "=&{v[244:247]}"(b), "=&{v[248:249]}"(d), [cnt] "+s"(cnt)
      : [va] "v"(va), [vb] "v"(vb), [vc] "v"(vc), [srd] "s"(c.srd), [so] "s"(so)
      : "v250", "vcc", "scc", "memory");
  c.budget = __builtin_amdgcn_readfirstlane(cnt);
}

constexpr unsigned WIMG = 128 * 288;
DEV v4u wfrag(LAS char* L, unsigned img, int lane, int rowbase, int colbyte, int cstep) {
  const unsigned o = img + (unsigned)(rowbase + 8 * (lane >> 4) + ((lane >> 2) & 3)) * 288u + (unsigned)(colbyte + (lane & 3) * cstep);
  return cat(ldtr(L, o), ldtr(L, o + 4 * 288));
}
template <int NCH, class SRC, class USE>
DEV void stage_weights(Ctx& c, SRC src, USE use) {
  constexpr int DEPTH = NCH < 5 ? NCH : 5;
  v4u st[NCH][4];
#pragma unroll
  for (int ch = 0; ch < DEPTH; ++ch)
#pragma unroll
    for (int m = 0; m < 4; ++m) st[ch][m] = *(const v4u*)src(ch, (c.tid >> 4) + 32 * m, c.tid & 15);
#pragma unroll
  for (int ch = 0; ch < NCH; ++ch) {
    const unsigned img = (ch & 1) * WIMG;
#pragma unroll
    for (int m = 0; m < 4; ++m) sts16(c.L, img + (unsigned)((c.tid >> 4) + 32 * m) * 288u + (unsigned)(c.tid & 15) * 16u, st[ch][m]);
    if (ch + DEPTH < NCH) {
#pragma unroll
      for (int m = 0; m < 4; ++m) st[ch + DEPTH][m] = *(const v4u*)src(ch + DEPTH, (c.tid >> 4) + 32 * m, c.tid & 15);
    }
    wg_barrier();
    use(ch, img);
  }
  wg_barrier();
}

DEV void ln_rows(Ctx& c, const v4u (&xr)[5], unsigned aimg, unsigned modl) {
  f32x2 v[20];
  f32x2 s2 = {0.f, 0.f};
#pragma unroll
  for (int m = 0; m < 5; ++m)
#pragma unroll
    for (int d = 0; d < 4; ++d) {
      const unsigned u = xr[m][d];
      v[4 * m + d] = f32x2{bflo(u), bfhi(u)};
      s2 += v[4 * m + d];
    }
  const float mean = sum16(s2[0] + s2[1]) * (1.f / DM);
  f32x2 q2 = {0.f, 0.f};
#pragma unroll
  for (int i = 0; i < 20; ++i) { v[i] -= mean; q2 = __builtin_elementwise_fma(v[i], v[i], q2); }
  const float rs = __builtin_amdgcn_rsqf(sum16(q2[0] + q2[1]) * (1.f / DM) + 1e-5f);
  const int row = 4 * c.wave + (c.lane >> 4), ci = c.lane & 15;
  const int tau = row >> 4, r = row & 15, g = ci & 3, k1 = (ci >> 2) & 1;
  const unsigned wbase = aimg + (unsigned)(tau * 20 + (ci >> 2)) * 1024u + (unsigned)g * 256u + (unsigned)((r ^ (k1 * 4 + g)) * 16);
#pragma unroll
  for (int m = 0; m < 5; ++m) {
    const unsigned mo = modl + (unsigned)(ci + 16 * m) * 32u;
    const f32x4 s0 = __builtin_bit_cast(f32x4, lds16(c.L, mo)), s1 = __builtin_bit_cast(f32x4, lds16(c.L, mo + 16));
    const f32x4 h0 = __builtin_bit_cast(f32x4, lds16(c.L, mo + 2560)), h1 = __builtin_bit_cast(f32x4, lds16(c.L, mo + 2560 + 16));
    const f32x2 y0 = __builtin_elementwise_fma(v[4 * m] * rs, s0.xy, h0.xy), y1 = __builtin_elementwise_fma(v[4 * m + 1] * rs, s0.zw, h0.zw);
    const f32x2 y2 = __builtin_elementwise_fma(v[4 * m + 2] * rs, s1.xy, h1.xy), y3 = __builtin_elementwise_fma(v[4 * m + 3] * rs, s1.zw, h1.zw);
    v4u o = {pk2(y0[0], y0[1]), pk2(y1[0], y1[1]), pk2(y2[0], y2[1]), pk2(y3[0], y3[1])};
    sts16(c.L, wbase + (unsigned)m * 4096u, o);
  }
}
DEV v4u afrag(Ctx& c, unsigned aimg, int tau, int kt) {
  const int g = c.lane >> 4, r = c.lane & 15;
  return lds16(c.L, aimg + (unsigned)(tau * 20 + kt) * 1024u + (unsigned)g * 256u + (unsigned)((r ^ ((kt & 1) * 4 + g)) * 16));
}
DEV void load_modl(Ctx& c, const Params& p, unsigned modl, int shift_idx, int scale_idx) {
  const float* mod = (const float*)(p.ws + MOD_OFF);
  for (int i = c.tid; i < DM; i += 512) {
    *(LAS float*)(c.L + modl + i * 4) = rbf(1.f + mod[scale_idx * DM + i]);
    *(LAS float*)(c.L + modl + 2560 + i * 4) = mod[shift_idx * DM + i];
  }
}
DEV void load_x_rows(Ctx& c, const u16* x, int seg, v4u (&xr)[5]) {
  const u16* src = x + (size_t)(seg * 32 + 4 * c.wave + (c.lane >> 4)) * DM + (c.lane & 15) * 8;
#pragma unroll
  for (int m = 0; m < 5; ++m) xr[m] = *(const v4u*)(src + 128 * m);
}

DEV void h_item(Ctx& c, const Params& p, int seg) {
  constexpr unsigned AIMG = 0, MODL = 40960;
  v4u xr[5];
  load_x_rows(c, p.x, seg, xr);
  load_modl(c, p, MODL, 0, 1);
  wg_barrier();
  ln_rows(c, xr, AIMG, MODL);
  wg_barrier();
#pragma unroll
  for (int m = 0; m < 5; ++m)
    stw16(c.ws, (unsigned)c.tid * 16u + m * 8192u, HI_OFF + (unsigned)seg * 40960u, lds16(c.L, AIMG + (unsigned)c.tid * 16u + m * 8192u));
  drain_vm();
  wg_barrier();
  if (c.tid == 0) set_prog(c, PG_H + (unsigned)seg * 128u, 0);
}
DEV void load_h(Ctx& c, int seg, v4u (&xr)[5]) {
#pragma unroll
  for (int m = 0; m < 5; ++m) xr[m] = ldw16(c.ws, (unsigned)c.tid * 16u + m * 8192u, HI_OFF + (unsigned)seg * 40960u);
}
DEV void put_h(Ctx& c, const v4u (&xr)[5]) {
#pragma unroll
  for (int m = 0; m < 5; ++m) sts16(c.L, (unsigned)c.tid * 16u + m * 8192u, xr[m]);
}

DEV void b_item(Ctx& c, const Params& p, int bi) {
  const int h = bi / 5, cb = bi - 5 * h;
  const int g = c.lane >> 4, col = c.lane & 15;
  {
    v4u st[4];
#pragma unroll
    for (int m = 0; m < 4; ++m)
      st[m] = *(const v4u*)(p.Wo + (size_t)(h * 128 + (c.tid >> 4) + 32 * m) * DM + cb * 128 + (c.tid & 15) * 8);
#pragma unroll
    for (int m = 0; m < 4; ++m) sts16(c.L, (unsigned)((c.tid >> 4) + 32 * m) * 288u + (unsigned)(c.tid & 15) * 16u, st[m]);
  }
  const u16* abase = p.Wv + (size_t)(8 * (col >> 2) + (col & 3)) * 4096 + h * 128 + 8 * g;
  v4u a[2][4];
#pragma unroll
  for (int tau = 0; tau < 2; ++tau)
#pragma unroll
    for (int ks = 0; ks < 4; ++ks) a[tau][ks] = *(const v4u*)(abase + (size_t)(32 * c.wave + 4 * tau) * 4096 + 32 * ks);
  wg_barrier();
  v4u bw[8][4];
#pragma unroll
  for (int ct = 0; ct < 8; ++ct)
#pragma unroll
    for (int ks = 0; ks < 4; ++ks) bw[ct][ks] = wfrag(c.L, 0, c.lane, 32 * ks, 32 * ct, 8);
  const unsigned vo = (unsigned)(col >> 3) * 327680u + (unsigned)(g * 16 + (h & 1) * 8 + (col & 7)) * 16u;
  for (int kt = c.wave; kt < 20; kt += 8) {
    v4u an[2][4];
    const int ktn = kt + 8 < 20 ? kt + 8 : kt;
#pragma unroll
    for (int tau = 0; tau < 2; ++tau)
#pragma unroll
      for (int ks = 0; ks < 4; ++ks) an[tau][ks] = *(const v4u*)(abase + (size_t)(32 * ktn + 4 * tau) * 4096 + 32 * ks);
#pragma unroll
    for (int ct = 0; ct < 8; ++ct) {
      f32x4 d0 = {0, 0, 0, 0}, d1 = {0, 0, 0, 0};
#pragma unroll
      for (int ks = 0; ks < 4; ++ks) { d0 = mfma(a[0][ks], bw[ct][ks], d0); d1 = mfma(a[1][ks], bw[ct][ks], d1); }
      v4u o = {pk2(d0[0], d0[1]), pk2(d0[2], d0[3]), pk2(d1[0], d1[1]), pk2(d1[2], d1[3])};
      stw16(c.ws, vo, WC_OFF + (unsigned)(16 * cb + 2 * ct) * 327680u + (unsigned)((h >> 1) * 20 + kt) * 1024u, o);
    }
#pragma unroll
    for (int tau = 0; tau < 2; ++tau)
#pragma unroll
      for (int ks = 0; ks < 4; ++ks) a[tau][ks] = an[tau][ks];
  }
  drain_vm();
  wg_barrier();
  if (c.tid == 0) set_prog(c, PG_B + (unsigned)(cb * 32 + h) * 128u, 0);
}

constexpr unsigned XM = 0;
DEV void stage_mem0(Ctx& c, const Params& p) {
  for (int e = c.tid; e < 8 * DM; e += 512) {
    const int i = e / DM, k = e - i * DM;
    const int line = k >> 3, pos = line ^ ((line >> 1) & 1);
    *(LAS u16*)(c.L + XM + pos * 128 + (k & 7) * 16 + i * 2) = p.mem0[e];
  }
}
DEV void project(Ctx& c, const v4u (&w)[2][20], f32x4& d0, f32x4& d1, int idx) {
  const int g = c.lane >> 4, q = (c.lane >> 2) & 3, pp = c.lane & 3;
  const unsigned rowo = (unsigned)((q & 1) + 4 * (q >> 1) + 2 * (pp >> 1)) * 16u + (unsigned)(pp & 1) * 8u;
  const unsigned o0 = XM + (unsigned)g * 256u + (unsigned)(g & 1) * 128u + rowo;
  const unsigned o1 = XM + (unsigned)g * 256u + (unsigned)((g & 1) ^ 1) * 128u + rowo;
  v4u a[10];
#pragma unroll
  for (int b = 0; b < 10; ++b) a[b] = cat(ldtr(c.L, o0 + b * 1024u), ldtr(c.L, o1 + b * 1024u));
  d0 = f32x4{0, 0, 0, 0}; d1 = f32x4{0, 0, 0, 0};
#pragma unroll
  for (int b = 0; b < 10; ++b) {
    d0 = smf64(a[b], w[0][2 * b], w[0][2 * b + 1], d0, idx);
    d1 = smf64(a[b], w[1][2 * b], w[1][2 * b + 1], d1, idx);
  }
}
DEV unsigned wait_phase(Ctx& c) {
  int v;
  for (;;) {
    asm volatile("" ::: "memory");
    v = __builtin_amdgcn_readfirstlane((int)ldw4(c.ws, PHASE_OFF, 0));
    if (v >= 0) break;
    if (c.budget == 0) { v = 0; break; }
    --c.budget;
    __builtin_amdgcn_s_sleep(2);
  }
  return (unsigned)v & 1u;
}

DEV void role_S(Ctx& c, const Params& p, int h, unsigned xcc) {
  constexpr unsigned XQ = 10240, XK = 12288, KXL = 16384;
  const unsigned ring = ring_off(p.ws);
  if (h == 0 && c.wave == 0) {
    unsigned best[2] = {~0u, ~0u};
    for (int r = 0; r < 8; ++r) {
      const unsigned o = ring + RING_BYTES + (unsigned)(r & 1) * 8192u;
      u64 t0, t1;
      asm volatile("s_waitcnt vmcnt(0) lgkmcnt(0)\n\ts_memtime %0\n\ts_waitcnt lgkmcnt(0)" : "=s"(t0)::"memory");
      if (c.lane == 0) stw4(c.ws, o, 0, 0u);
      asm volatile("s_waitcnt vmcnt(0)\n\ts_memtime %0\n\ts_waitcnt lgkmcnt(0)" : "=s"(t1)::"memory");
      const unsigned d = (unsigned)(t1 - t0);
      if (r & 1) best[1] = d < best[1] ? d : best[1]; else best[0] = d < best[0] ? d : best[0];
    }
    if (c.lane == 0) set_prog(c, PHASE_OFF, best[1] < best[0] ? 1 : 0);
  }
  v4u w[2][20];
  {
    const int wq = c.wave & 3, isk = c.wave >> 2;
    stage_weights<10>(
        c, [&](int ch, int row, int c16) { return (row < 64 ? p.Wq : p.Wk) + (size_t)(64 * ch + (row & 63)) * 4096 + h * 128 + c16 * 8; },
        [&](int ch, unsigned img) {
#pragma unroll
          for (int t = 0; t < 2; ++t)
#pragma unroll
            for (int k2 = 0; k2 < 2; ++k2) w[t][2 * ch + k2] = wfrag(c.L, img, c.lane, isk * 64 + 32 * k2, (32 * wq + 16 * t) * 2, 8);
        });
  }
  stage_mem0(c, p);
  const unsigned phase = wait_phase(c);
  const int idx = (c.lane & 8) ? (int)0xEEEEEEEE : 0x44444444;
  const int g = c.lane >> 4, r16 = c.lane & 15, i8 = c.lane & 7, ih = (c.lane >> 3) & 1;
  const unsigned va = ring + remap(RMS_L + (unsigned)c.wave * 1280u + (unsigned)c.lane * 16u, phase);
  const unsigned vb = ring + remap(RMS_L + (unsigned)c.wave * 1280u + 1024u + (unsigned)r16 * 16u, phase);
  const unsigned pt_o = ring + remap(RP_L + (unsigned)h * 512u + (unsigned)((ih * 2 + (g & 1)) * 8 + i8) * 16u + (unsigned)(g >> 1) * 8u, phase);
  const unsigned pm_o = ring + remap(RP_L + 16384u + (unsigned)h * 128u + (unsigned)((g & 1) * 8 + i8) * 8u, phase);
  const unsigned tro = (unsigned)(8 * g + ((c.lane >> 2) & 3)) * 16u + (unsigned)(c.lane & 1) * 8u;
  const int tok = 8 * (r16 >> 3) + 4 * ((r16 >> 1) & 1) + 2 * ((r16 >> 2) & 1) + (r16 & 1);
  const unsigned kx_o = (unsigned)(g * 16 + tok) * 16u;
  int qk_seen = -1;
  for (int s = 0; s < NSEG; ++s) {
    if (c.wave == 1 && s < NSEG - 1) {
      if (qk_seen < s) { wait_prog(c, PG_QK + (unsigned)h * 128u, 1, s); qk_seen = s; }
      v4u kx[8];
#pragma unroll
      for (int f = 0; f < 8; ++f) kx[f] = ldw16(c.ws, kx_o + f * 1024u, KX_OFF + (unsigned)(s * 32 + h) * 8192u);
#pragma unroll
      for (int f = 0; f < 8; ++f) sts16(c.L, KXL + (unsigned)(s & 1) * 8192u + f * 1024u + (unsigned)c.lane * 16u, kx[f]);
    }
    if (s > 0) {
      v4u a, b;
      poll2(c, va, vb, (unsigned)(s & 3) * RM_STR, a, b);
      sts16(c.L, XM + (unsigned)c.wave * 1280u + (unsigned)c.lane * 16u, a);
      if (c.lane < 16) sts16(c.L, XM + (unsigned)c.wave * 1280u + 1024u + (unsigned)c.lane * 16u, b);
    }
    wg_barrier();
    f32x4 d0, d1;
    project(c, w, d0, d1, idx);
    {
      const float z0 = swapadd32(d0[0], d1[0]), z1 = swapadd32(d0[1], d1[1]), z2 = swapadd32(d0[2], d1[2]), z3 = swapadd32(d0[3], d1[3]);
      v2u o = {pk2(z0, z1), pk2(z2, z3)};
      sts8(c.L, XQ + (unsigned)c.wave * 512u + (unsigned)((c.lane >> 5) * 16 + r16) * 16u + (unsigned)(g & 1) * 8u, o);
    }
    if (c.wave == 0) {
      v4u kx[8];
#pragma unroll
      for (int f = 0; f < 8; ++f) kx[f] = lds16(c.L, KXL + (unsigned)(s & 1) * 8192u + f * 1024u + (unsigned)c.lane * 16u);
      wg_barrier();
      if (s < NSEG - 1) {
        f32x4 sm = {0, 0, 0, 0}, s0 = {0, 0, 0, 0}, s1 = {0, 0, 0, 0};
#pragma unroll
        for (int ks = 0; ks < 4; ++ks) {
          const v4u qf = cat(ldtr(c.L, XQ + tro + ks * 512u), ldtr(c.L, XQ + tro + ks * 512u + 64u));
          const v4u kf = cat(ldtr(c.L, XK + tro + ks * 512u), ldtr(c.L, XK + tro + ks * 512u + 64u));
          s0 = mfma(kx[ks], qf, s0); s1 = mfma(kx[4 + ks], qf, s1); sm = mfma(kf, qf, sm);
        }
        float t[4];
#pragma unroll
        for (int r = 0; r < 4; ++r) t[r] = ih ? s1[r] : s0[r];
        float mx = fmaxf(fmaxf(fmaxf(t[0], t[1]), fmaxf(t[2], t[3])), fmaxf(fmaxf(sm[0], sm[1]), fmaxf(sm[2], sm[3])));
        mx = fmaxf(mx, dppf<0x128>(mx));
        mx = max_x16(mx); mx = max_x32(mx);
        constexpr float CS = 0.08838834764831845f * 1.4426950408889634f;
        const float mo = -mx * CS;
        float e[4], f[4];
#pragma unroll
        for (int r = 0; r < 4; ++r) { e[r] = __builtin_amdgcn_exp2f(t[r] * CS + mo); f[r] = __builtin_amdgcn_exp2f(sm[r] * CS + mo); }
        float sum = (e[0] + e[1]) + (e[2] + e[3]) + 0.25f * ((f[0] + f[1]) + (f[2] + f[3]));
        sum += dppf<0x128>(sum);
        sum = add_x16(sum); sum = add_x32(sum);
        const float inv = __builtin_amdgcn_rcpf(sum);
        v2u pt = {nosent(pk2(e[0] * inv, e[1] * inv)), nosent(pk2(e[2] * inv, e[3] * inv))};
        v2u pm = {nosent(pk2(f[0] * inv, f[1] * inv)), nosent(pk2(f[2] * inv, f[3] * inv))};
        const unsigned so = (unsigned)(s & 3) * RP_STR;
        stw8(c.ws, pt_o, so, pt);
        if (g < 2 && ih == 0) stw8(c.ws, pm_o, so, pm);
        const unsigned sr = (unsigned)((s + 2) & 3) * RP_STR;
        const v2u ones = {~0u, ~0u};
        stw8(c.ws, pt_o, sr, ones);
        if (g < 2 && ih == 0) stw8(c.ws, pm_o, sr, ones);
      }
    } else {
      wg_barrier();
      if (c.wave == 7) {
#pragma unroll
        for (int ks = 0; ks < 4; ++ks) {
          const v4u kf = cat(ldtr(c.L, XK + tro + ks * 512u), ldtr(c.L, XK + tro + ks * 512u + 64u));
          if (ih == 0) stw16(c.ws, (unsigned)(g * 8 + i8) * 16u + ks * 512u, KM_OFF + (unsigned)(s * 32 + h) * 2048u, kf);
        }
        drain_vm();
        if (c.lane == 0) set_prog(c, PG_KM + (unsigned)h * 128u, s);
      }
    }
  }
}

DEV void role_C(Ctx& c, const Params& p, int ci) {
  constexpr unsigned ZL = 10240, RED = 16384;
  const unsigned ring = ring_off(p.ws);
  v4u w[2][20];
  wait_prog(c, PG_B + (unsigned)((ci >> 4) * 32 + 4 * c.wave) * 128u, 4, 0);
#pragma unroll
  for (int t = 0; t < 2; ++t)
#pragma unroll
    for (int kt = 0; kt < 20; ++kt)
      w[t][kt] = ldw16(c.ws, (unsigned)c.lane * 16u + (unsigned)(t * 20 + kt) * 1024u, WC_OFF + (unsigned)ci * 327680u + (unsigned)c.wave * 40960u);
  stage_mem0(c, p);
  const unsigned phase = wait_phase(c);
  const int idx = (c.lane & 8) ? (int)0xEEEEEEEE : 0x44444444;
  const int g = c.lane >> 4, r16 = c.lane & 15, cc = c.lane & 7, hp = (c.lane >> 3) & 1;
  const unsigned va = ring + remap(RMC_L + (unsigned)c.wave * 1280u + (unsigned)c.lane * 16u, phase);
  const unsigned vb = ring + remap(RMC_L + (unsigned)c.wave * 1280u + 1024u + (unsigned)r16 * 16u, phase);
  const unsigned pa = ring + remap(RP_L + (unsigned)c.wave * 2048u + (unsigned)c.lane * 16u, phase);
  const unsigned pb = pa + 1024u;
  const unsigned pc = ring + remap(RP_L + 16384u + (unsigned)c.wave * 512u + (unsigned)c.lane * 8u, phase);
  const unsigned mpos = (unsigned)(ci ^ ((ci >> 1) & 1)) * 128u + (unsigned)cc * 16u + (unsigned)hp * 8u;
  const unsigned ms_o = ring + remap(RMS_L + mpos, phase), mc_o = ring + remap(RMC_L + mpos, phase);
  const int gbz = 2 * (c.lane >> 5) + hp;
  int vu_seen = -1;
  for (int s = 0; s < NSEG; ++s) {
    if (s > 0) {
      v4u a, b;
      poll2(c, va, vb, (unsigned)(s & 3) * RM_STR, a, b);
      sts16(c.L, XM + (unsigned)c.wave * 1280u + (unsigned)c.lane * 16u, a);
      if (c.lane < 16) sts16(c.L, XM + (unsigned)c.wave * 1280u + 1024u + (unsigned)c.lane * 16u, b);
    }
    wg_barrier();
    v4u u[4];
    if (s < NSEG - 1) {
      if (vu_seen < s) { wait_prog(c, PG_VU + (unsigned)(4 * c.wave) * 128u, 4, s); vu_seen = s; }
#pragma unroll
      for (int j = 0; j < 4; ++j)
        u[j] = ldw16(c.ws, (unsigned)(g * 8 + cc) * 16u + j * 512u, UC_OFF + (unsigned)(s * 80 + ci) * 16384u + (unsigned)c.wave * 2048u);
    }
    f32x4 d0, d1;
    project(c, w, d0, d1, idx);
    v4u zf;
    {
      const float z0 = swapadd32(d0[0], d1[0]), z1 = swapadd32(d0[1], d1[1]), z2 = swapadd32(d0[2], d1[2]), z3 = swapadd32(d0[3], d1[3]);
      v2u o = {pk2(z0, z1), pk2(z2, z3)};
      stw8(c.ws, (unsigned)(gbz * 8 + cc) * 16u + (unsigned)(g & 1) * 8u, ZC_OFF + (unsigned)(s * 80 + ci) * 4096u + (unsigned)c.wave * 512u, o);
      const unsigned zo = ZL + (unsigned)c.wave * 512u + (unsigned)(gbz * 8 + cc) * 16u + (unsigned)(g & 1) * 4u;
      *(LAS unsigned*)(c.L + zo) = o.x;
      *(LAS unsigned*)(c.L + zo + 8) = o.y;
      wait_lds();
      zf = lds16(c.L, ZL + (unsigned)c.wave * 512u + (unsigned)(g * 8 + cc) * 16u);
    }
    if (s < NSEG - 1) {
      v4u a, b; v2u d;
      asm volatile("" ::"v"(u[0]), "v"(u[1]), "v"(u[2]), "v"(u[3]), "v"(zf));
      poll3(c, pa, pb, pc, (unsigned)(s & 3) * RP_STR, a, b, d);
      f32x4 e0 = {0, 0, 0, 0}, e1 = {0, 0, 0, 0};
      e0 = smf64(a, u[0], u[1], e0, idx);
      e1 = smf32(d, zf, e1, idx);
      e0 = smf64(b, u[2], u[3], e0, idx);
      e0 += e1;
      if (hp == 0) sts16(c.L, RED + (unsigned)((c.wave * 4 + g) * 8 + cc) * 16u, __builtin_bit_cast(v4u, e0));
    } else {
      drain_vm();
    }
    wg_barrier();
    if (c.wave == 0) {
      if (s < NSEG - 1) {
        const unsigned ro = RED + (unsigned)((g * 8 + hp) * 8 + cc) * 16u;
        const f32x4 a0 = __builtin_bit_cast(f32x4, lds16(c.L, ro)), a1 = __builtin_bit_cast(f32x4, lds16(c.L, ro + 256));
        const f32x4 a2 = __builtin_bit_cast(f32x4, lds16(c.L, ro + 512)), a3 = __builtin_bit_cast(f32x4, lds16(c.L, ro + 768));
        f32x4 t = (a0 + a1) + (a2 + a3);
        float y[4];
#pragma unroll
        for (int r = 0; r < 4; ++r) { const float v = t[r]; y[r] = add_x32(add_x16(v)); }
        v2u o = {nosent(pk2(y[0], y[1])), nosent(pk2(y[2], y[3]))};
        const unsigned so = (unsigned)((s + 1) & 3) * RM_STR, sr = (unsigned)((s + 3) & 3) * RM_STR;
        const v2u ones = {~0u, ~0u};
        if (g == 0) { stw8(c.ws, ms_o, so, o); stw8(c.ws, mc_o, so, o); stw8(c.ws, ms_o, sr, ones); stw8(c.ws, mc_o, sr, ones); }
      }
    } else if (c.wave == 1) {
      if (c.lane == 0) set_prog(c, PG_Z + (unsigned)ci * 128u, s);
    }
    if (s < NSEG - 1) __builtin_amdgcn_s_sleep(16);
  }
}

DEV void role_QK(Ctx& c, const Params& p, int h) {
  constexpr unsigned AIMG = 0, QKR = 40960;
  v4u w[2][20];
  const int wq = c.wave & 3, isk = c.wave >> 2;
  h_item(c, p, 2 * h);
  stage_weights<10>(
      c, [&](int ch, int row, int c16) { return (row < 64 ? p.Wq : p.Wk) + (size_t)(64 * ch + (row & 63)) * 4096 + h * 128 + c16 * 8; },
      [&](int ch, unsigned img) {
#pragma unroll
        for (int t = 0; t < 2; ++t)
#pragma unroll
          for (int k2 = 0; k2 < 2; ++k2) w[t][2 * ch + k2] = wfrag(c.L, img, c.lane, isk * 64 + 32 * k2, (32 * wq + 4 * t) * 2, 16);
      });
  v4u xr[5];
  wait_prog(c, PG_H, 64, 0);
  load_h(c, 0, xr);
  const int g = c.lane >> 4, r16 = c.lane & 15;
  for (int t = 0; t < NSEG + QW; ++t) {
    if (t < NSEG) {
      put_h(c, xr);
      wg_barrier();
      load_h(c, t + 1 < NSEG ? t + 1 : t, xr);
      f32x4 acc[2][2];
#pragma unroll
      for (int a = 0; a < 2; ++a)
#pragma unroll
        for (int b = 0; b < 2; ++b) acc[a][b] = f32x4{0, 0, 0, 0};
#pragma unroll
      for (int kt = 0; kt < 20; ++kt) {
        const v4u a0 = afrag(c, AIMG, 0, kt), a1 = afrag(c, AIMG, 1, kt);
        acc[0][0] = mfma(w[0][kt], a0, acc[0][0]); acc[1][0] = mfma(w[1][kt], a0, acc[1][0]);
        acc[0][1] = mfma(w[0][kt], a1, acc[0][1]); acc[1][1] = mfma(w[1][kt], a1, acc[1][1]);
      }
#pragma unroll
      for (int tau = 0; tau < 2; ++tau) {
        v4u o = {pk2(acc[0][tau][0], acc[0][tau][1]), pk2(acc[0][tau][2], acc[0][tau][3]), pk2(acc[1][tau][0], acc[1][tau][1]),
                 pk2(acc[1][tau][2], acc[1][tau][3])};
        sts16(c.L, QKR + (unsigned)(t & 3) * 16384u + (unsigned)(isk * 8 + tau * 4 + wq) * 1024u + (unsigned)c.lane * 16u, o);
        if (isk) stw16(c.ws, (unsigned)c.lane * 16u, KX_OFF + (unsigned)(t * 32 + h) * 8192u + (unsigned)(tau * 4 + wq) * 1024u, o);
      }
      if (isk) drain_vm();
      wg_barrier();
      if (c.tid == 256) set_prog(c, PG_QK + (unsigned)h * 128u, t);
    }
    if (t >= QW && c.wave < 2) {
      const int s = t - QW, qt = c.wave;
      const unsigned sl = QKR + (unsigned)(s & 3) * 16384u;
      f32x4 sm = {0, 0, 0, 0}, s0 = {0, 0, 0, 0}, s1 = {0, 0, 0, 0};
      v4u qf[4];
      const int tk = 8 * (r16 >> 2) + (r16 & 3);
      const unsigned ko = sl + 8192u + (unsigned)(tk >> 4) * 4096u + (unsigned)(g * 16 + (tk & 15)) * 16u;
#pragma unroll
      for (int ks = 0; ks < 4; ++ks) {
        qf[ks] = lds16(c.L, sl + (unsigned)(qt * 4 + ks) * 1024u + (unsigned)c.lane * 16u);
        s0 = mfma(lds16(c.L, ko + ks * 1024u), qf[ks], s0);
        s1 = mfma(lds16(c.L, ko + 64u + ks * 1024u), qf[ks], s1);
      }
      wait_prog(c, PG_KM + (unsigned)h * 128u, 1, s);
#pragma unroll
      for (int ks = 0; ks < 4; ++ks)
        sm = mfma(ldw16(c.ws, (unsigned)(g * 8 + (r16 & 7)) * 16u + ks * 512u, KM_OFF + (unsigned)(s * 32 + h) * 2048u), qf[ks], sm);
      constexpr float SC = 0.08838834764831845f, L2E = 1.4426950408889634f;
      const int qi = 16 * qt + r16;
      float v[12];
#pragma unroll
      for (int r = 0; r < 4; ++r) {
        v[r] = (8 * g + r <= qi) ? s0[r] * SC : -1e9f;
        v[4 + r] = (8 * g + 4 + r <= qi) ? s1[r] * SC : -1e9f;
        v[8 + r] = sm[r] * SC;
      }
      float mx = v[0];
#pragma unroll
      for (int i = 1; i < 12; ++i) mx = fmaxf(mx, v[i]);
      mx = max_x16(mx); mx = max_x32(mx);
      float st = 0.f, sk = 0.f;
#pragma unroll
      for (int i = 0; i < 12; ++i) { v[i] = __builtin_amdgcn_exp2f((v[i] - mx) * L2E); if (i < 8) st += v[i]; else sk += v[i]; }
      float sum = st + 0.5f * sk;
      sum = add_x16(sum); sum = add_x32(sum);
      const float inv = __builtin_amdgcn_rcpf(sum);
      v4u pt = {pk2(v[0] * inv, v[1] * inv), pk2(v[2] * inv, v[3] * inv), pk2(v[4] * inv, v[5] * inv), pk2(v[6] * inv, v[7] * inv)};
      v2u pm = {pk2(v[8] * inv, v[9] * inv), pk2(v[10] * inv, v[11] * inv)};
      stw16(c.ws, (unsigned)c.lane * 16u, PT_OFF + (unsigned)((s * 2 + qt) * 32 + h) * 1024u, pt);
      if (g < 2) stw8(c.ws, (unsigned)r16 * 16u + (unsigned)g * 8u, PM_OFF + (unsigned)((s * 2 + qt) * 32 + h) * 256u, pm);
      drain_vm();
      if (c.lane == 0) set_prog(c, PG_P + (unsigned)(qt * 32 + h) * 128u, s);
    }
  }
}

DEV void role_VU(Ctx& c, const Params& p, int h) {
  constexpr unsigned AIMG = 0, VIMG = 40960;
  v4u wv[20], wo[5][4];
  h_item(c, p, 2 * h + 1);
  stage_weights<10>(
      c,
      [&](int ch, int row, int c16) {
        return ch < 5 ? p.Wv + (size_t)(128 * ch + row) * 4096 + h * 128 + c16 * 8 : p.Wo + (size_t)(h * 128 + row) * DM + (ch - 5) * 128 + c16 * 8;
      },
      [&](int ch, unsigned img) {
#pragma unroll
        for (int k4 = 0; k4 < 4; ++k4) {
          const v4u f = wfrag(c.L, img, c.lane, 32 * k4, 32 * c.wave, 8);
          if (ch < 5) wv[4 * ch + k4] = f; else wo[ch - 5][k4] = f;
        }
      });
  v4u xr[5];
  wait_prog(c, PG_H, 64, 0);
  load_h(c, 0, xr);
  const int g = c.lane >> 4, r16 = c.lane & 15;
  const int tk = 8 * (r16 >> 2) + (r16 & 3);
  const unsigned vro = VIMG + (unsigned)(tk >> 4) * 4096u + (unsigned)(g * 16 + (tk & 15)) * 16u;
  const unsigned vwo = VIMG + (unsigned)(c.wave >> 1) * 1024u + (unsigned)((2 * (c.wave & 1) + (g >> 1)) * 16 + r16) * 16u + (unsigned)(g & 1) * 8u;
  const unsigned uo = (unsigned)(r16 >> 3) * 16384u + (unsigned)(g * 8 + (r16 & 7)) * 16u;
  for (int t = 0; t < NSEG; ++t) {
    put_h(c, xr);
    drain_vm();
    wg_barrier();
    if (t > 0 && c.tid == 0) set_prog(c, PG_VU + (unsigned)h * 128u, t - 1);
    load_h(c, t + 1 < NSEG ? t + 1 : t, xr);
    f32x4 a0 = {0, 0, 0, 0}, a1 = {0, 0, 0, 0};
#pragma unroll
    for (int kt = 0; kt < 20; ++kt) { a0 = mfma(wv[kt], afrag(c, AIMG, 0, kt), a0); a1 = mfma(wv[kt], afrag(c, AIMG, 1, kt), a1); }
    sts8(c.L, vwo, v2u{pk2(a0[0], a0[1]), pk2(a0[2], a0[3])});
    sts8(c.L, vwo + 4096u, v2u{pk2(a1[0], a1[1]), pk2(a1[2], a1[3])});
    wg_barrier();
    v4u vf[2][4];
#pragma unroll
    for (int tau = 0; tau < 2; ++tau)
#pragma unroll
      for (int ks = 0; ks < 4; ++ks) vf[tau][ks] = lds16(c.L, vro + tau * 64u + ks * 1024u);
#pragma unroll
    for (int m = 0; m < 5; ++m) {
      f32x4 d0 = {0, 0, 0, 0}, d1 = {0, 0, 0, 0};
#pragma unroll
      for (int ks = 0; ks < 4; ++ks) { d0 = mfma(vf[0][ks], wo[m][ks], d0); d1 = mfma(vf[1][ks], wo[m][ks], d1); }
      v4u o = {pk2(d0[0], d0[1]), pk2(d0[2], d0[3]), pk2(d1[0], d1[1]), pk2(d1[2], d1[3])};
      stw16(c.ws, uo, UC_OFF + (unsigned)(t * 80 + 2 * (8 * m + c.wave)) * 16384u + (unsigned)h * 512u, o);
    }
  }
  drain_vm();
  wg_barrier();
  if (c.tid == 0) set_prog(c, PG_VU + (unsigned)h * 128u, NSEG - 1);
}

DEV void red_write(Ctx& c, unsigned R, const f32x4 (&d)[4]) {
  const int g = c.lane >> 4, tok = c.lane & 15;
#pragma unroll
  for (int ct = 0; ct < 4; ++ct)
    sts16(c.L, R + (unsigned)c.wave * 4096u + (unsigned)tok * 256u + (unsigned)(((ct * 4 + g) ^ (tok & 7)) * 16), __builtin_bit_cast(v4u, d[ct]));
}
DEV f32x4 red_read(Ctx& c, unsigned R) {
  const int tok = 4 * c.wave + (c.lane >> 4), qd = c.lane & 15;
  const unsigned o = R + (unsigned)tok * 256u + (unsigned)((qd ^ (tok & 7)) * 16);
  f32x4 t = __builtin_bit_cast(f32x4, lds16(c.L, o));
#pragma unroll
  for (int w = 1; w < 8; ++w) t += __builtin_bit_cast(f32x4, lds16(c.L, o + w * 4096u));
  return t;
}

DEV void role_OUT(Ctx& c, const Params& p, int o) {
  const int qt = o & 1, cbk = o >> 1;
  const int g = c.lane >> 4, r16 = c.lane & 15;
  const float* mod = (const float*)(p.ws + MOD_OFF);
  const f32x4 ga = *(const f32x4*)(mod + 2 * DM + 64 * cbk + 4 * r16);
  const unsigned uo = (unsigned)(r16 >> 3) * 16384u + (unsigned)(g * 8 + (r16 & 7)) * 16u;
  const unsigned zo = (unsigned)(r16 >> 3) * 4096u + (unsigned)(g * 8 + (r16 & 7)) * 16u;
  for (int s = 0; s < NSEG; ++s) {
    v4u u[4][4], z[4], pf[5];
    wait_prog(c, PG_VU + (unsigned)(4 * c.wave) * 128u, 4, s);
#pragma unroll
    for (int ct = 0; ct < 4; ++ct)
#pragma unroll
      for (int j = 0; j < 4; ++j)
        u[ct][j] = ldw16(c.ws, uo + j * 512u, UC_OFF + (unsigned)(s * 80 + 8 * cbk + 2 * ct) * 16384u + (unsigned)c.wave * 2048u);
    const int row = s * 32 + 16 * qt + 4 * (c.wave & 3) + g;
    const v2u xv = *(const v2u*)(p.x + (size_t)row * DM + 64 * cbk + 4 * r16);
    wait_prog(c, PG_Z + (unsigned)(8 * cbk) * 128u, 8, s);
#pragma unroll
    for (int ct = 0; ct < 4; ++ct) z[ct] = ldw16(c.ws, zo, ZC_OFF + (unsigned)(s * 80 + 8 * cbk + 2 * ct) * 4096u + (unsigned)c.wave * 512u);
    wait_prog(c, PG_P + (unsigned)(qt * 32 + 4 * c.wave) * 128u, 4, s);
#pragma unroll
    for (int j = 0; j < 4; ++j) pf[j] = ldw16(c.ws, (unsigned)c.lane * 16u + j * 1024u, PT_OFF + (unsigned)((s * 2 + qt) * 32 + 4 * c.wave) * 1024u);
    pf[4] = ldw16(c.ws, (unsigned)c.lane * 16u, PM_OFF + (unsigned)((s * 2 + qt) * 8 + c.wave) * 1024u);
    f32x4 d[4];
#pragma unroll
    for (int ct = 0; ct < 4; ++ct) {
      d[ct] = f32x4{0, 0, 0, 0};
#pragma unroll
      for (int j = 0; j < 4; ++j) d[ct] = mfma(u[ct][j], pf[j], d[ct]);
      d[ct] = mfma(z[ct], pf[4], d[ct]);
    }
    red_write(c, 0, d);
    wg_barrier();
    if (c.wave < 4) {
      const f32x4 t = red_read(c, 0);
      v2u ov = {pk2(bflo(xv.x) + rbf(ga[0] * rbf(t[0])), bfhi(xv.x) + rbf(ga[1] * rbf(t[1]))),
                pk2(bflo(xv.y) + rbf(ga[2] * rbf(t[2])), bfhi(xv.y) + rbf(ga[3] * rbf(t[3])))};
      stw8(c.ws, (unsigned)row * 1280u + (unsigned)(64 * cbk + 4 * r16) * 2u, X1_OFF, ov);
      drain_vm();
    }
    wg_barrier();
    if (c.tid == 0) set_prog(c, PG_X1 + (unsigned)(qt * 10 + cbk) * 128u, s);
  }
}

DEV void role_F1(Ctx& c, const Params& p, int fi) {
  constexpr unsigned AIMG = 0, MODL = 40960;
  v4u wg[20], wu[20];
  const int wt = c.wave & 3, tau = c.wave >> 2;
  stage_weights<5>(
      c, [&](int ch, int row, int c16) { return (c16 < 8 ? p.w1 : p.w3) + (size_t)(128 * ch + row) * FF + 64 * fi + (c16 & 7) * 8; },
      [&](int ch, unsigned img) {
#pragma unroll
        for (int k4 = 0; k4 < 4; ++k4) {
          wg[4 * ch + k4] = wfrag(c.L, img, c.lane, 32 * k4, 32 * wt, 8);
          wu[4 * ch + k4] = wfrag(c.L, img, c.lane, 32 * k4, 128 + 32 * wt, 8);
        }
      });
  load_modl(c, p, MODL, 3, 4);
  wg_barrier();
  const int g = c.lane >> 4, r16 = c.lane & 15;
  const unsigned ho = (unsigned)(2 * fi + (wt >> 1)) * 1024u + (unsigned)((2 * (wt & 1) + (g >> 1)) * 16 + r16) * 16u + (unsigned)(g & 1) * 8u;
  for (int s = 0; s < NSEG; ++s) {
    v4u xr[5];
    wait_prog(c, PG_X1 + (unsigned)(tau * 10) * 128u, 10, s);
#pragma unroll
    for (int m = 0; m < 5; ++m)
      xr[m] = ldw16(c.ws, (unsigned)(4 * c.wave + g) * 1280u + (unsigned)r16 * 16u + m * 256u, X1_OFF + (unsigned)s * 40960u);
    ln_rows(c, xr, AIMG, MODL);
    wg_barrier();
    f32x4 a = {0, 0, 0, 0}, b = {0, 0, 0, 0};
#pragma unroll
    for (int kt = 0; kt < 20; ++kt) { const v4u f = afrag(c, AIMG, tau, kt); a = mfma(wg[kt], f, a); b = mfma(wu[kt], f, b); }
    float y[4];
#pragma unroll
    for (int r = 0; r < 4; ++r) {
      const float gt = rbf(a[r]);
      const float sl = rbf(gt * __builtin_amdgcn_rcpf(1.f + __builtin_amdgcn_exp2f(-1.4426950408889634f * gt)));
      y[r] = sl * rbf(b[r]);
    }
    stw8(c.ws, ho, HF_OFF + (unsigned)(s * 2 + tau) * 55296u, v2u{pk2(y[0], y[1]), pk2(y[2], y[3])});
    drain_vm();
    wg_barrier();
    if (c.tid == 0) set_prog(c, PG_HF + (unsigned)fi * 128u, s);
  }
}

DEV void role_F2(Ctx& c, const Params& p, int o) {
  const int tau = o & 1, cbk = o >> 1;
  const int g = c.lane >> 4, r16 = c.lane & 15;
  const int k0 = 7 * c.wave;
  v4u w[7][4];
  {
    const unsigned img = (unsigned)c.wave * 5120u;
    v4u st[7][4];
#pragma unroll
    for (int j = 0; j < 7; ++j) {
      const int kt = k0 + j < 54 ? k0 + j : 53;
#pragma unroll
      for (int m = 0; m < 4; ++m) st[j][m] = *(const v4u*)(p.w2 + (size_t)(32 * kt + (c.lane >> 3) + 8 * m) * DM + 64 * cbk + (c.lane & 7) * 8);
    }
#pragma unroll
    for (int j = 0; j < 7; ++j) {
#pragma unroll
      for (int m = 0; m < 4; ++m) sts16(c.L, img + (unsigned)((c.lane >> 3) + 8 * m) * 160u + (unsigned)(c.lane & 7) * 16u, st[j][m]);
      wait_lds();
#pragma unroll
      for (int ct = 0; ct < 4; ++ct) {
        const unsigned a = img + (unsigned)(8 * g + ((c.lane >> 2) & 3)) * 160u + (unsigned)(32 * ct + (c.lane & 3) * 8);
        w[j][ct] = cat(ldtr(c.L, a), ldtr(c.L, a + 4 * 160));
        if (k0 + j >= 54) w[j][ct] = v4u{0, 0, 0, 0};
      }
      wait_lds();
    }
  }
  wg_barrier();
  const float* mod = (const float*)(p.ws + MOD_OFF);
  const f32x4 gb = *(const f32x4*)(mod + 5 * DM + 64 * cbk + 4 * r16);
  const int f0 = k0 >> 1, nf = (c.wave == 7) ? 3 : 4;
  for (int s = 0; s < NSEG; ++s) {
    v4u hf[7];
    wait_prog(c, PG_HF + (unsigned)f0 * 128u, nf, s);
#pragma unroll
    for (int j = 0; j < 7; ++j) {
      const int kt = k0 + j < 54 ? k0 + j : 53;
      hf[j] = ldw16(c.ws, (unsigned)c.lane * 16u, HF_OFF + (unsigned)((s * 2 + tau) * 54 + kt) * 1024u);
    }
    const int row = s * 32 + 16 * tau + 4 * (c.wave & 3) + g;
    const v2u xv = ldw8(c.ws, (unsigned)row * 1280u + (unsigned)(64 * cbk + 4 * r16) * 2u, X1_OFF);
    f32x4 d[4];
#pragma unroll
    for (int ct = 0; ct < 4; ++ct) {
      d[ct] = f32x4{0, 0, 0, 0};
#pragma unroll
      for (int j = 0; j < 7; ++j) d[ct] = mfma(w[j][ct], hf[j], d[ct]);
    }
    const unsigned R = (unsigned)(s & 1) * 32768u;
    red_write(c, R, d);
    wg_barrier();
    if (c.wave < 4) {
      const f32x4 t = red_read(c, R);
      v2u ov = {pk2(bflo(xv.x) + rbf(gb[0] * rbf(t[0])), bfhi(xv.x) + rbf(gb[1] * rbf(t[1]))),
                pk2(bflo(xv.y) + rbf(gb[2] * rbf(t[2])), bfhi(xv.y) + rbf(gb[3] * rbf(t[3])))};
      *(v2u*)(p.out + (size_t)row * DM + 64 * cbk + 4 * r16) = ov;
    }
  }
}

__global__ void __launch_bounds__(512, 2) k_main(Params p) {
  extern __shared__ __attribute__((aligned(16))) char lds[];
  Ctx c;
  c.L = (LAS char*)lds;
  c.tid = threadIdx.x; c.lane = c.tid & 63; c.wave = __builtin_amdgcn_readfirstlane(c.tid >> 6);
  c.ws = __builtin_amdgcn_make_buffer_rsrc(p.ws, 0, WS_END, 0x00020000);
  c.srd = v4u{(unsigned)(u64)p.ws, (unsigned)((u64)p.ws >> 32) & 0xffffu, WS_END, 0x00020000u};
  c.budget = SPIN_BUDGET;
  const unsigned xcc = (unsigned)__builtin_amdgcn_s_getreg(20 | (3 << 11)) & 7u;
  if (c.wave == 0) {
    int role = 0;
    int t = 0;
    if (c.lane == 0) t = (int)__hip_atomic_fetch_add((unsigned*)(p.ws + TICK_OFF + xcc * 128u), 1u, __ATOMIC_RELAXED, __HIP_MEMORY_SCOPE_AGENT) + 1;
    t = __builtin_amdgcn_readfirstlane(t);
    if (t < 32) {
      role = (int)xcc * 32 + t;
    } else {
      int n[8], tot;
      for (;;) {
        asm volatile("" ::: "memory");
        const int v = (int)ldw4(c.ws, TICK_OFF + (unsigned)(c.lane & 7) * 128u, 0) + 1;
        tot = 0;
#pragma unroll
        for (int x = 0; x < 8; ++x) { n[x] = __builtin_amdgcn_readlane(v, x); tot += n[x]; }
        if (tot >= 256 || c.budget == 0) break;
        --c.budget;
        __builtin_amdgcn_s_sleep(8);
      }
      int rank = t - 32;
#pragma unroll
      for (int x = 0; x < 8; ++x) if (x < (int)xcc && n[x] > 32) rank += n[x] - 32;
      role = -1;
#pragma unroll
      for (int x = 0; x < 8; ++x) {
        const int fr = n[x] < 32 ? 32 - n[x] : 0;
        if (role < 0 && rank < fr) role = x * 32 + n[x] + rank;
        if (role < 0) rank -= fr;
      }
    }
    if (c.lane == 0) *(LAS int*)(c.L + LDS_BYTES - 16) = role;
  }
  wg_barrier();
  const int role = __builtin_amdgcn_readfirstlane(*(LAS int*)(c.L + LDS_BYTES - 16));
  if (role < 0) return;
  const int x = role >> 5, t = role & 31;
  if (x == 0) { role_S(c, p, t, xcc); return; }
  if (x == 4) { role_QK(c, p, t); return; }
  if (x == 5) { role_VU(c, p, t); return; }
  int ci = -1, pool = -1;
  if (x < 4) {
    const int nc = x == 3 ? 26 : 27;
    if (t < nc) ci = (x - 1) * 27 + t; else pool = (x - 1) * 5 + (t - nc);
  } else {
    pool = 16 + (x - 6) * 32 + t;
  }
  b_item(c, p, ci >= 0 ? ci : 80 + pool);
  if (ci >= 0) { role_C(c, p, ci); return; }
  if (pool < 13) return;
  if (pool >= 16 && pool < 43) { role_F1(c, p, pool - 16); return; }
  if (pool >= 48 && pool < 68) { role_OUT(c, p, pool - 48); return; }
  role_F2(c, p, pool < 16 ? pool + 4 : (pool < 48 ? pool - 31 : pool - 68));
}

__global__ void __launch_bounds__(256) k_init(Params p) {
  const int tid = threadIdx.x;
  if (blockIdx.x < 60) {
    __shared__ float red[32][65];
    const int kg = tid >> 3, cq = tid & 7;
    v4u wv[20];
#pragma unroll
    for (int i = 0; i < 20; ++i) wv[i] = *(const v4u*)(p.wmod + (size_t)(kg + 32 * i) * 3840 + blockIdx.x * 64 + cq * 8);
    float acc[8] = {0, 0, 0, 0, 0, 0, 0, 0};
#pragma unroll
    for (int i = 0; i < 20; ++i) {
      const float cv = bflo(p.c[kg + 32 * i]);
      const float sv = rbf(cv / (1.f + __expf(-cv)));
#pragma unroll
      for (int d = 0; d < 4; ++d) { acc[2 * d] += sv * bflo(wv[i][d]); acc[2 * d + 1] += sv * bfhi(wv[i][d]); }
    }
#pragma unroll
    for (int j = 0; j < 8; ++j) red[kg][cq * 8 + j] = acc[j];
    __syncthreads();
    if (tid < 64) {
      float s = 0.f;
#pragma unroll
      for (int k = 0; k < 32; ++k) s += red[k][tid];
      ((float*)(p.ws + MOD_OFF))[blockIdx.x * 64 + tid] = rbf(s);
    }
  } else {
    const rsrc_t ws = __builtin_amdgcn_make_buffer_rsrc(p.ws, 0, WS_END, 0x00020000);
    const unsigned o = (unsigned)((blockIdx.x - 60) * 256 + tid) * 16u;
    const v4u ones = {~0u, ~0u, ~0u, ~0u};
    stw16(ws, o < CTRL_BYTES ? o : ring_off(p.ws) + (o - CTRL_BYTES), 0, ones);
  }
}

}

extern "C" void kernel_launch(void* const* d_in, const int* in_sizes, int n_in, void* d_out, int out_size, void* d_ws, size_t ws_size,
                              hipStream_t stream) {
  Params p;
  p.x = (const u16*)d_in[0]; p.mem0 = (const u16*)d_in[1]; p.Wq = (const u16*)d_in[2]; p.Wk = (const u16*)d_in[3];
  p.Wv = (const u16*)d_in[4]; p.Wo = (const u16*)d_in[5]; p.w1 = (const u16*)d_in[8]; p.w3 = (const u16*)d_in[9];
  p.w2 = (const u16*)d_in[10]; p.c = (const u16*)d_in[11]; p.wmod = (const u16*)d_in[12];
  p.out = (u16*)d_out; p.ws = (char*)d_ws;
  (void)hipFuncSetAttribute((const void*)k_main, hipFuncAttributeMaxDynamicSharedMemorySize, LDS_BYTES);
  k_init<<<60 + (CTRL_BYTES + RING_BYTES) / 4096, 256, 0, stream>>>(p);
  k_main<<<256, 512, LDS_BYTES, stream>>>(p);
}
```

```cpp
#include <hip/hip_runtime.h>
#include <hip/hip_bf16.h>
#include <stdint.h>

namespace {

typedef __bf16 bf16x2 __attribute__((ext_vector_type(2)));
typedef __bf16 bf16x4 __attribute__((ext_vector_type(4)));
typedef __bf16 bf16x8 __attribute__((ext_vector_type(8)));
typedef __bf16 bf16x16 __attribute__((ext_vector_type(16)));
typedef short s16x4 __attribute__((ext_vector_type(4)));
typedef short s16x8 __attribute__((ext_vector_type(8)));
typedef float f32x4 __attribute__((ext_vector_type(4)));
typedef float f32x2 __attribute__((ext_vector_type(2)));
typedef unsigned v4u __attribute__((ext_vector_type(4)));
typedef unsigned v2u __attribute__((ext_vector_type(2)));
typedef unsigned long long u64;
typedef unsigned short u16;
#define LAS __attribute__((address_space(3)))
#define DEV __device__ __forceinline__
template <int V> struct IC { static constexpr int value = V; };

constexpr int NSEG = 64, DM = 640, NH = 32, FF = 1728;
constexpr int AHEAD = 5;

constexpr unsigned CTRL_BYTES = 49152;
constexpr unsigned TICK_OFF = 0;
constexpr unsigned PHASE_OFF = 1024;
constexpr unsigned PG_QK = 2048;
constexpr unsigned PG_VU = PG_QK + 32 * 128;
constexpr unsigned PG_KM = PG_VU + 32 * 128;
constexpr unsigned PG_Z = PG_KM + 32 * 128;
constexpr unsigned PG_P = PG_Z + 80 * 128;
constexpr unsigned PG_X1 = PG_P + 64 * 128;
constexpr unsigned PG_HF = PG_X1 + 20 * 128;
constexpr unsigned PG_H = PG_HF + 28 * 128;
static_assert(PG_H + 64 * 128 <= CTRL_BYTES, "ctrl");
constexpr unsigned MOD_OFF = CTRL_BYTES;
constexpr unsigned RING_MIN = 131072;
constexpr unsigned RING_BYTES = 458752;
constexpr unsigned RP_L = 0, RP_STR = 49152;
constexpr unsigned RMS_L = 98304, RMC_L = 163840, RM_STR = 32768;
constexpr unsigned QK_OFF = 2u << 20;
constexpr unsigned UC_OFF = QK_OFF + 64u * 32 * 16384;
constexpr unsigned WC_OFF = UC_OFF + 64u * 80 * 16384;
constexpr unsigned KM_OFF = WC_OFF + 80u * 327680;
constexpr unsigned ZC_OFF = KM_OFF + 64u * 32 * 2048;
constexpr unsigned PT_OFF = ZC_OFF + 64u * 80 * 4096;
constexpr unsigned PM_OFF = PT_OFF + 64u * 2 * 32 * 1024;
constexpr unsigned X1_OFF = PM_OFF + 64u * 2 * 8 * 1024;
constexpr unsigned HF_OFF = X1_OFF + 2048u * 1280;
constexpr unsigned HI_OFF = HF_OFF + 64u * 2 * 54 * 1024;
constexpr unsigned WS_END = HI_OFF + 64u * 40960;
static_assert(WS_END <= (256u << 20), "workspace");

constexpr int LDS_BYTES = 122880;
constexpr unsigned SPIN_BUDGET = 1u << 21;

struct Params {
  const u16* x; const u16* mem0; const u16* Wq; const u16* Wk; const u16* Wv; const u16* Wo;
  const u16* w1; const u16* w3; const u16* w2; const u16* c; const u16* wmod;
  u16* out; char* ws;
};

DEV unsigned pk2(float a, float b) { bf16x2 v = {(__bf16)a, (__bf16)b}; return __builtin_bit_cast(unsigned, v); }
DEV float bflo(unsigned u) { return __uint_as_float(u << 16); }
DEV float bfhi(unsigned u) { return __uint_as_float(u & 0xffff0000u); }
DEV float rbf(float a) { return (float)(__bf16)a; }
DEV unsigned nosent(unsigned u) { return u < 0xfffffffeu ? u : 0xfffffffeu; }
DEV void wg_barrier() { asm volatile("s_waitcnt lgkmcnt(0)\n\ts_barrier" ::: "memory"); }
DEV void drain_vm() { asm volatile("s_waitcnt vmcnt(0)" ::: "memory"); }
DEV void wait_lds() { asm volatile("s_waitcnt lgkmcnt(0)" ::: "memory"); }
DEV unsigned remap(unsigned l, unsigned phase) { return ((l >> 13) << 14) | (phase << 13) | (l & 8191u); }
DEV unsigned ring_off(const char* ws) {
  const u64 a = (u64)ws;
  return (unsigned)(((a + RING_MIN + 524287ull) & ~524287ull) - a);
}
typedef __amdgpu_buffer_rsrc_t rsrc_t;
DEV v4u ldw16(rsrc_t r, unsigned vo, unsigned so) { return __builtin_amdgcn_raw_buffer_load_b128(r, vo, so, 16); }
DEV v2u ldw8(rsrc_t r, unsigned vo, unsigned so) { return __builtin_amdgcn_raw_buffer_load_b64(r, vo, so, 16); }
DEV unsigned ldw4(rsrc_t r, unsigned vo, unsigned so) { return __builtin_amdgcn_raw_buffer_load_b32(r, vo, so, 16); }
DEV void stw16(rsrc_t r, unsigned vo, unsigned so, v4u v) { __builtin_amdgcn_raw_buffer_store_b128(v, r, vo, so, 16); }
DEV void stw8(rsrc_t r, unsigned vo, unsigned so, v2u v) { __builtin_amdgcn_raw_buffer_store_b64(v, r, vo, so, 16); }
DEV void stw4(rsrc_t r, unsigned vo, unsigned so, unsigned v) { __builtin_amdgcn_raw_buffer_store_b32(v, r, vo, so, 16); }
DEV v4u lds16(LAS char* L, unsigned o) { return *(LAS v4u*)(L + o); }
DEV void sts16(LAS char* L, unsigned o, v4u v) { *(LAS v4u*)(L + o) = v; }
DEV void sts8(LAS char* L, unsigned o, v2u v) { *(LAS v2u*)(L + o) = v; }
DEV v2u ldtr(LAS char* L, unsigned o) {
  return __builtin_bit_cast(v2u, __builtin_amdgcn_ds_read_tr16_b64_v4bf16((LAS bf16x4*)(L + o)));
}
DEV v4u cat(v2u a, v2u b) { v4u r = {a.x, a.y, b.x, b.y}; return r; }
DEV f32x4 mfma(v4u a, v4u b, f32x4 c) {
  return __builtin_amdgcn_mfma_f32_16x16x32_bf16(__builtin_bit_cast(bf16x8, a), __builtin_bit_cast(bf16x8, b), c, 0, 0, 0);
}
DEV f32x4 smf64(v4u a, v4u b0, v4u b1, f32x4 c, int idx) {
  bf16x16 b = __builtin_shufflevector(__builtin_bit_cast(bf16x8, b0), __builtin_bit_cast(bf16x8, b1), 0, 1, 2, 3, 4, 5, 6, 7, 8, 9, 10,
                                      11, 12, 13, 14, 15);
  return __builtin_amdgcn_smfmac_f32_16x16x64_bf16(__builtin_bit_cast(bf16x8, a), b, c, idx, 0, 0);
}
DEV f32x4 smf32(v2u a, v4u b, f32x4 c, int idx) {
  return __builtin_amdgcn_smfmac_f32_16x16x32_bf16(__builtin_bit_cast(s16x4, a), __builtin_bit_cast(s16x8, b), c, idx, 0, 0);
}
typedef unsigned v8u __attribute__((ext_vector_type(8)));
DEV v8u cat8(v4u a, v4u b) { return __builtin_shufflevector(a, b, 0, 1, 2, 3, 4, 5, 6, 7); }
DEV void smf64_o(f32x4& c, v4u a, v8u b, int idx) {
  asm volatile("v_smfmac_f32_16x16x64_bf16 %0, %1, %2, %3" : "+v"(c) : "v"(a), "v"(b), "v"(idx) : "memory");
}
DEV void mfma_o(f32x4& c, v4u a, v4u b) { asm volatile("v_mfma_f32_16x16x32_bf16 %0, %1, %2, %0" : "+v"(c) : "v"(a), "v"(b) : "memory"); }
DEV void mfma_o0(f32x4& c, v4u a, v4u b) { asm volatile("v_mfma_f32_16x16x32_bf16 %0, %1, %2, 0" : "=&v"(c) : "v"(a), "v"(b) : "memory"); }
DEV void mfma_done(f32x4& a, f32x4& b) { asm volatile("s_nop 7" : "+v"(a), "+v"(b)); }
template <int CTRL> DEV float dppf(float v) {
  return __int_as_float(__builtin_amdgcn_update_dpp(0, __float_as_int(v), CTRL, 0xf, 0xf, true));
}
DEV float sum16(float v) {
  v += dppf<0xb1>(v); v += dppf<0x4e>(v); v += dppf<0x141>(v); v += dppf<0x140>(v);
  return v;
}
DEV float fmx(float a, float b) { return __builtin_elementwise_maximum(a, b); }
#define SB() __builtin_amdgcn_sched_barrier(0)
DEV float max_x16(float v) { unsigned u = __float_as_uint(v); auto r = __builtin_amdgcn_permlane16_swap(u, u, false, false); unsigned a = r[0], b = r[1]; return fmx(__uint_as_float(a), __uint_as_float(b)); }
DEV float max_x32(float v) { unsigned u = __float_as_uint(v); auto r = __builtin_amdgcn_permlane32_swap(u, u, false, false); unsigned a = r[0], b = r[1]; return fmx(__uint_as_float(a), __uint_as_float(b)); }
DEV float add_x16(float v) { unsigned u = __float_as_uint(v); auto r = __builtin_amdgcn_permlane16_swap(u, u, false, false); unsigned a = r[0], b = r[1]; return __uint_as_float(a) + __uint_as_float(b); }
DEV float add_x32(float v) { unsigned u = __float_as_uint(v); auto r = __builtin_amdgcn_permlane32_swap(u, u, false, false); unsigned a = r[0], b = r[1]; return __uint_as_float(a) + __uint_as_float(b); }
DEV float swapadd32(float a, float b) {
  auto r = __builtin_amdgcn_permlane32_swap(__float_as_uint(a), __float_as_uint(b), false, false);
  unsigned x = r[0], y = r[1];
  return __uint_as_float(x) + __uint_as_float(y);
}

struct Ctx {
  rsrc_t ws;
  v4u srd;
  LAS char* L;
  int tid, lane, wave;
  unsigned budget;
};

DEV void wait_words(Ctx& c, unsigned vo, int need) {
  for (;;) {
    asm volatile("" ::: "memory");
    const int v = (int)ldw4(c.ws, vo, 0);
    if (__all(v >= need)) break;
    if (c.budget == 0) break;
    --c.budget;
    __builtin_amdgcn_s_sleep(1);
  }
  asm volatile("" ::: "memory");
}
DEV void wait_prog(Ctx& c, unsigned off, int n, int need) { wait_words(c, off + (unsigned)(c.lane < n ? c.lane : 0) * 128u, need); }
DEV void wait_seen(Ctx& c, unsigned off, int n, int need, int& seen) {
  if (seen >= need) return;
  const unsigned vo = off + (unsigned)((c.lane & 3) < n ? (c.lane & 3) : 0) * 128u;
  for (;;) {
    asm volatile("" ::: "memory");
    const int v = (int)ldw4(c.ws, vo, 0);
    const int a0 = __builtin_amdgcn_readlane(v, 0), a1 = __builtin_amdgcn_readlane(v, 1), a2 = __builtin_amdgcn_readlane(v, 2), a3 = __builtin_amdgcn_readlane(v, 3);
    seen = min(min(a0, a1), min(a2, a3));
    if (seen >= need) break;
    if (c.budget == 0) break;
    --c.budget;
    __builtin_amdgcn_s_sleep(1);
  }
  asm volatile("" ::: "memory");
}
DEV int peek_req(Ctx& c, unsigned off, int n) { return (int)ldw4(c.ws, off + (unsigned)((c.lane & 3) < n ? (c.lane & 3) : 0) * 128u, 0); }
DEV void peek_use(int v, int& seen) {
  const int a0 = __builtin_amdgcn_readlane(v, 0), a1 = __builtin_amdgcn_readlane(v, 1), a2 = __builtin_amdgcn_readlane(v, 2), a3 = __builtin_amdgcn_readlane(v, 3);
  seen = max(seen, min(min(a0, a1), min(a2, a3)));
  asm volatile("" ::: "memory");
}
#define STAMP(row, v) do { } while (0)
#define CYC(t) do { } while (0)
#define RTS(t) do { } while (0)
#define PUTV(row, v, x) do { } while (0)
DEV void set_prog(Ctx& c, unsigned off, int v) { stw4(c.ws, off, 0, (unsigned)v); }

#define P_LDA "buffer_load_dwordx4 v[240:243], %[va], %[srd], %[so] offen sc1\n\t"
#define P_LDB "buffer_load_dwordx4 v[244:247], %[vb], %[srd], %[so] offen sc1\n\t"
#define P_LDC "buffer_load_dwordx2 v[248:249], %[vc], %[srd], %[so] offen sc1\n\t"
#define P_CKA "v_max3_u32 v250, v240, v241, v242\n\tv_max_u32 v250, v250, v243\n\tv_cmp_eq_u32 vcc, -1, v250\n\t"
#define P_CKB "v_max3_u32 v250, v244, v245, v246\n\tv_max_u32 v250, v250, v247\n\tv_cmp_eq_u32 vcc, -1, v250\n\t"
#define P_CKC "v_max_u32 v250, v248, v249\n\tv_cmp_eq_u32 vcc, -1, v250\n\t"
#define P_BUD "s_sub_u32 %[cnt], %[cnt], 1\n\ts_cbranch_scc1 98f\n\t"
#define P_END "s_branch 99f\n\t98:\n\ts_mov_b32 %[cnt], 0\n\t99:\n\ts_waitcnt vmcnt(0)\n\t"
DEV void poll2(Ctx& c, unsigned va, unsigned vb, unsigned so, v4u& a, v4u& b) {
  unsigned cnt = __builtin_amdgcn_readfirstlane(c.budget);
  asm volatile(
      "s_waitcnt vmcnt(0)\n\ts_nop 4\n\t" P_LDA P_LDB
      "10:\n\t"
      "s_waitcnt vmcnt(1)\n\t" P_CKA "s_cbranch_vccz 21f\n\t" P_LDA
      "s_waitcnt vmcnt(1)\n\t" P_CKB "s_cbranch_vccz 20f\n\t" P_LDB P_BUD "s_branch 10b\n\t"
      "20:\n\t"
      "s_waitcnt vmcnt(0)\n\t" P_CKA "s_cbranch_vccz 99f\n\t" P_LDA P_BUD "s_branch 20b\n\t"
      "21:\n\t"
      "s_waitcnt vmcnt(0)\n\t" P_CKB "s_cbranch_vccz 99f\n\t" P_LDB P_BUD "s_branch 21b\n\t" P_END
      : "=&{v[240:243]}"(a), "=&{v[244:247]}"(b), [cnt] "+s"(cnt)
      : [va] "v"(va), [vb] "v"(vb), [srd] "s"(c.srd), [so] "s"(so)
      : "v250", "vcc", "scc", "memory");
  c.budget = __builtin_amdgcn_readfirstlane(cnt);
}
#define P_MA "v_smfmac_f32_16x16x64_bf16 %[ea], v[240:243], %[u01], %[idx]\n\t"
#define P_MB "v_smfmac_f32_16x16x64_bf16 %[eb], v[244:247], %[u23], %[idx]\n\t"
#define P_MC "v_smfmac_f32_16x16x32_bf16 %[ec], v[248:249], %[zf], %[idx]\n\t"
DEV void poll3(Ctx& c, unsigned va, unsigned vb, unsigned vc, unsigned so, v8u u01, v8u u23, v4u zf, int idx, f32x4& ea, f32x4& eb, f32x4& ec) {
  unsigned cnt = __builtin_amdgcn_readfirstlane(c.budget);
  asm volatile(
      "s_waitcnt vmcnt(0)\n\ts_nop 4\n\t" P_LDA P_LDB P_LDC
      "10:\n\t"
      "s_waitcnt vmcnt(2)\n\t" P_CKA "s_cbranch_vccz 41f\n\t" P_LDA
      "s_waitcnt vmcnt(2)\n\t" P_CKB "s_cbranch_vccz 42f\n\t" P_LDB
      "s_waitcnt vmcnt(2)\n\t" P_CKC "s_cbranch_vccz 43f\n\t" P_LDC P_BUD "s_branch 10b\n\t"
      "42:\n\t" P_MB
      "32:\n\t"
      "s_waitcnt vmcnt(1)\n\t" P_CKC "s_cbranch_vccz 50f\n\t" P_LDC
      "s_waitcnt vmcnt(1)\n\t" P_CKA "s_cbranch_vccz 54f\n\t" P_LDA P_BUD "s_branch 32b\n\t"
      "43:\n\t" P_MC
      "33:\n\t"
      "s_waitcnt vmcnt(1)\n\t" P_CKA "s_cbranch_vccz 52f\n\t" P_LDA
      "s_waitcnt vmcnt(1)\n\t" P_CKB "s_cbranch_vccz 51f\n\t" P_LDB P_BUD "s_branch 33b\n\t"
      "50:\n\t" P_MC "s_branch 20f\n\t"
      "51:\n\t" P_MB
      "20:\n\t"
      "s_waitcnt vmcnt(0)\n\t" P_CKA "s_cbranch_vccz 60f\n\t" P_LDA P_BUD "s_branch 20b\n\t"
      "60:\n\t" P_MA "s_branch 99f\n\t"
      "52:\n\t" P_MA "s_branch 21f\n\t"
      "53:\n\t" P_MC
      "21:\n\t"
      "s_waitcnt vmcnt(0)\n\t" P_CKB "s_cbranch_vccz 61f\n\t" P_LDB P_BUD "s_branch 21b\n\t"
      "61:\n\t" P_MB "s_branch 99f\n\t"
      "54:\n\t" P_MA "s_branch 22f\n\t"
      "98:\n\ts_mov_b32 %[cnt], 0\n\ts_waitcnt vmcnt(0)\n\ts_branch 99f\n\t"
      "41:\n\t" P_MA
      "31:\n\t"
      "s_waitcnt vmcnt(1)\n\t" P_CKB "s_cbranch_vccz 55f\n\t" P_LDB
      "s_waitcnt vmcnt(1)\n\t" P_CKC "s_cbranch_vccz 53b\n\t" P_LDC
      "s_sub_u32 %[cnt], %[cnt], 1\n\ts_cbranch_scc1 98b\n\ts_branch 31b\n\t"
      "55:\n\t" P_MB
      "22:\n\t"
      "s_waitcnt vmcnt(0)\n\t" P_CKC "s_cbranch_vccz 62f\n\t" P_LDC
      "s_sub_u32 %[cnt], %[cnt], 1\n\ts_cbranch_scc1 98b\n\ts_branch 22b\n\t"
      "62:\n\t" P_MC
      "99:\n\ts_nop 7\n\t"
      : [ea] "+v"(ea), [eb] "+v"(eb), [ec] "+v"(ec), [cnt] "+s"(cnt)
      : [va] "v"(va), [vb] "v"(vb), [vc] "v"(vc), [srd] "s"(c.srd), [so] "s"(so), [u01] "v"(u01), [u23] "v"(u23), [zf] "v"(zf), [idx] "v"(idx)
      : "v240", "v241", "v242", "v243", "v244", "v245", "v246", "v247", "v248", "v249", "v250", "vcc", "scc", "memory");
  c.budget = __builtin_amdgcn_readfirstlane(cnt);
}

constexpr unsigned WIMG = 128 * 288;
DEV v4u wfrag(LAS char* L, unsigned img, int lane, int rowbase, int colbyte, int cstep) {
  const unsigned o = img + (unsigned)(rowbase + 8 * (lane >> 4) + ((lane >> 2) & 3)) * 288u + (unsigned)(colbyte + (lane & 3) * cstep);
  return cat(ldtr(L, o), ldtr(L, o + 4 * 288));
}
template <int NCH, class SRC, class USE, class MID>
DEV void stage_weights(Ctx& c, SRC src, USE use, MID mid) {
  constexpr int DEPTH = NCH < 5 ? NCH : 5;
  v4u st[NCH][4];
#pragma unroll
  for (int ch = 0; ch < DEPTH; ++ch)
#pragma unroll
    for (int m = 0; m < 4; ++m) st[ch][m] = *(const v4u*)src(ch, (c.tid >> 4) + 32 * m, c.tid & 15);
  asm volatile("" ::: "memory");
  mid();
#pragma unroll
  for (int ch = 0; ch < NCH; ++ch) {
    const unsigned img = (ch & 1) * WIMG;
#pragma unroll
    for (int m = 0; m < 4; ++m) sts16(c.L, img + (unsigned)((c.tid >> 4) + 32 * m) * 288u + (unsigned)(c.tid & 15) * 16u, st[ch][m]);
    if (ch + DEPTH < NCH) {
#pragma unroll
      for (int m = 0; m < 4; ++m) st[ch + DEPTH][m] = *(const v4u*)src(ch + DEPTH, (c.tid >> 4) + 32 * m, c.tid & 15);
    }
    wg_barrier();
    use(ch, img);
  }
  wg_barrier();
}

template <int NCH, class SRC, class USE>
DEV void stage_weights(Ctx& c, SRC src, USE use) { stage_weights<NCH>(c, src, use, [] {}); }

DEV void ln_rows(Ctx& c, const v4u (&xr)[5], unsigned aimg, unsigned modl) {
  f32x2 v[20];
  f32x2 s2 = {0.f, 0.f};
#pragma unroll
  for (int m = 0; m < 5; ++m)
#pragma unroll
    for (int d = 0; d < 4; ++d) {
      const unsigned u = xr[m][d];
      v[4 * m + d] = f32x2{bflo(u), bfhi(u)};
      s2 += v[4 * m + d];
    }
  const float mean = sum16(s2[0] + s2[1]) * (1.f / DM);
  f32x2 q2 = {0.f, 0.f};
#pragma unroll
  for (int i = 0; i < 20; ++i) { v[i] -= mean; q2 = __builtin_elementwise_fma(v[i], v[i], q2); }
  const float rs = __builtin_amdgcn_rsqf(sum16(q2[0] + q2[1]) * (1.f / DM) + 1e-5f);
  const int row = 4 * c.wave + (c.lane >> 4), ci = c.lane & 15;
  const int tau = row >> 4, r = row & 15, g = ci & 3, k1 = (ci >> 2) & 1;
  const unsigned wbase = aimg + (unsigned)(tau * 20 + (ci >> 2)) * 1024u + (unsigned)g * 256u + (unsigned)((r ^ (k1 * 4 + g)) * 16);
#pragma unroll
  for (int m = 0; m < 5; ++m) {
    const unsigned mo = modl + (unsigned)(ci + 16 * m) * 32u;
    const f32x4 s0 = __builtin_bit_cast(f32x4, lds16(c.L, mo)), s1 = __builtin_bit_cast(f32x4, lds16(c.L, mo + 16));
    const f32x4 h0 = __builtin_bit_cast(f32x4, lds16(c.L, mo + 2560)), h1 = __builtin_bit_cast(f32x4, lds16(c.L, mo + 2560 + 16));
    const f32x2 y0 = __builtin_elementwise_fma(v[4 * m] * rs, s0.xy, h0.xy), y1 = __builtin_elementwise_fma(v[4 * m + 1] * rs, s0.zw, h0.zw);
    const f32x2 y2 = __builtin_elementwise_fma(v[4 * m + 2] * rs, s1.xy, h1.xy), y3 = __builtin_elementwise_fma(v[4 * m + 3] * rs, s1.zw, h1.zw);
    v4u o = {pk2(y0[0], y0[1]), pk2(y1[0], y1[1]), pk2(y2[0], y2[1]), pk2(y3[0], y3[1])};
    sts16(c.L, wbase + (unsigned)m * 4096u, o);
  }
}
DEV v4u afrag(Ctx& c, unsigned aimg, int tau, int kt) {
  const int g = c.lane >> 4, r = c.lane & 15;
  return lds16(c.L, aimg + (unsigned)(tau * 20 + kt) * 1024u + (unsigned)g * 256u + (unsigned)((r ^ ((kt & 1) * 4 + g)) * 16));
}
DEV void load_modl(Ctx& c, const Params& p, unsigned modl, int shift_idx, int scale_idx) {
  const float* mod = (const float*)(p.ws + MOD_OFF);
  for (int i = c.tid; i < DM; i += 512) {
    *(LAS float*)(c.L + modl + i * 4) = rbf(1.f + mod[scale_idx * DM + i]);
    *(LAS float*)(c.L + modl + 2560 + i * 4) = mod[shift_idx * DM + i];
  }
}
DEV void load_x_rows(Ctx& c, const u16* x, int seg, v4u (&xr)[5]) {
  const u16* src = x + (size_t)(seg * 32 + 4 * c.wave + (c.lane >> 4)) * DM + (c.lane & 15) * 8;
#pragma unroll
  for (int m = 0; m < 5; ++m) xr[m] = *(const v4u*)(src + 128 * m);
}

constexpr unsigned H_IMG = 2 * WIMG, H_MODL = H_IMG + 40960;
static_assert(H_MODL + 5120 <= LDS_BYTES - 16, "lds");
DEV void h_begin(Ctx& c, const Params& p, int seg, v4u (&xr)[5]) {
  load_x_rows(c, p.x, seg, xr);
  load_modl(c, p, H_MODL, 0, 1);
}
DEV void h_finish(Ctx& c, int seg, const v4u (&xr)[5]) {
  wg_barrier();
  ln_rows(c, xr, H_IMG, H_MODL);
  wg_barrier();
#pragma unroll
  for (int m = 0; m < 5; ++m)
    stw16(c.ws, (unsigned)c.tid * 16u, HI_OFF + (unsigned)seg * 40960u + m * 8192u, lds16(c.L, H_IMG + (unsigned)c.tid * 16u + m * 8192u));
  drain_vm();
  wg_barrier();
  if (c.tid == 0) set_prog(c, PG_H + (unsigned)seg * 128u, 0);
}
DEV void wait_h_all(Ctx& c) {
  if (c.wave == 0) {
    wait_prog(c, PG_H, 64, 0);
    __builtin_amdgcn_fence(__ATOMIC_ACQUIRE, "agent");
    drain_vm();
  }
}
DEV void dma_h(Ctx& c, int seg, int m, unsigned aimg) {
  const unsigned dst = __builtin_amdgcn_readfirstlane(aimg + (unsigned)c.wave * 1024u + (unsigned)m * 8192u);
  const unsigned so = __builtin_amdgcn_readfirstlane(HI_OFF + (unsigned)seg * 40960u + (unsigned)m * 8192u);
  unsigned keep;
  asm volatile("s_mov_b32 %0, m0\n\ts_mov_b32 m0, %2\n\ts_nop 4\n\tbuffer_load_dwordx4 %1, %3, %4 offen sc1 lds\n\ts_mov_b32 m0, %0"
               : "=&s"(keep) : "v"((unsigned)c.tid * 16u), "s"(dst), "s"(c.srd), "s"(so) : "memory");
}

DEV void b_item(Ctx& c, const Params& p, int bi) {
  const int h = bi / 5, rb = bi - 5 * h;
  const int g = c.lane >> 4, col = c.lane & 15;
  const unsigned vo = (unsigned)(col >> 3) * 4096u + (unsigned)(h & 1) * 2048u + (unsigned)(g * 8 + (col & 7)) * 16u;
  auto drow = [](int r) { return (r & ~31) + 8 * ((r >> 2) & 3) + 4 * ((r >> 4) & 1) + (r & 3); };
  v4u af[4][2][4];
  stage_weights<6>(
      c,
      [&](int ch, int row, int c16) {
        return ch == 0 ? p.Wv + (size_t)(128 * rb + drow(row)) * 4096 + h * 128 + c16 * 8 : p.Wo + (size_t)(h * 128 + row) * DM + (ch - 1) * 128 + c16 * 8;
      },
      [&](int ch, unsigned img) {
        if (ch == 0) {
#pragma unroll
          for (int kt = 0; kt < 4; ++kt)
#pragma unroll
            for (int tau = 0; tau < 2; ++tau)
#pragma unroll
              for (int ks = 0; ks < 4; ++ks) af[kt][tau][ks] = lds16(c.L, img + (unsigned)(32 * kt + 16 * tau + col) * 288u + (unsigned)(64 * ks + 16 * g));
        } else {
          v4u bw[4];
#pragma unroll
          for (int ks = 0; ks < 4; ++ks) bw[ks] = wfrag(c.L, img, c.lane, 32 * ks, 32 * c.wave, 8);
#pragma unroll
          for (int kt = 0; kt < 4; ++kt) {
            f32x4 d0 = {0, 0, 0, 0}, d1 = {0, 0, 0, 0};
#pragma unroll
            for (int ks = 0; ks < 4; ++ks) { d0 = mfma(af[kt][0][ks], bw[ks], d0); d1 = mfma(af[kt][1][ks], bw[ks], d1); }
            v4u o = {pk2(d0[0], d0[1]), pk2(d0[2], d0[3]), pk2(d1[0], d1[1]), pk2(d1[2], d1[3])};
            stw16(c.ws, vo, WC_OFF + (unsigned)((h >> 1) * 5 + rb) * 327680u + (unsigned)(16 * (ch - 1) + 2 * c.wave) * 4096u + (unsigned)kt * 512u, o);
          }
        }
      });
}

constexpr unsigned XM = 0;
DEV void stage_mem0(Ctx& c, const Params& p) {
  for (int e = c.tid; e < 8 * DM; e += 512) {
    const int i = e / DM, k = e - i * DM;
    const int line = k >> 3, pos = line ^ ((line >> 1) & 1);
    *(LAS u16*)(c.L + XM + pos * 128 + (k & 7) * 16 + i * 2) = p.mem0[e];
  }
}
template <int AH>
DEV void project(Ctx& c, const v4u (&w)[2][20], f32x4& d0, f32x4& d1, int idx) {
  const int g = c.lane >> 4, q = (c.lane >> 2) & 3, pp = c.lane & 3;
  const unsigned rowo = (unsigned)((q & 1) + 4 * (q >> 1) + 2 * (pp >> 1)) * 16u + (unsigned)(pp & 1) * 8u;
  const unsigned o0 = XM + (unsigned)g * 256u + (unsigned)(g & 1) * 128u + rowo;
  const unsigned o1 = XM + (unsigned)g * 256u + (unsigned)((g & 1) ^ 1) * 128u + rowo;
  v4u a[10];
#pragma unroll
  for (int b = 0; b < AH; ++b) { a[b] = cat(ldtr(c.L, o0 + b * 1024u), ldtr(c.L, o1 + b * 1024u)); asm volatile("" ::: "memory"); }
#pragma unroll
  for (int b = 0; b < 10; ++b) {
    smf64_o(d0, a[b], cat8(w[0][2 * b], w[0][2 * b + 1]), idx);
    if (b + AH < 10) a[b + AH] = cat(ldtr(c.L, o0 + (b + AH) * 1024u), ldtr(c.L, o1 + (b + AH) * 1024u));
    smf64_o(d1, a[b], cat8(w[1][2 * b], w[1][2 * b + 1]), idx);
  }
}
DEV unsigned wait_phase(Ctx& c) {
  int v;
  for (;;) {
    asm volatile("" ::: "memory");
    v = __builtin_amdgcn_readfirstlane((int)ldw4(c.ws, PHASE_OFF, 0));
    if (v >= 0) break;
    if (c.budget == 0) { v = 0; break; }
    --c.budget;
    __builtin_amdgcn_s_sleep(2);
  }
  return (unsigned)v & 1u;
}

DEV void pick_phase(Ctx& c, const Params& p) {
  const unsigned ring = ring_off(p.ws);
  unsigned best[2] = {~0u, ~0u};
  for (int r = 0; r < 10; ++r) {
    const unsigned o = ring + RING_BYTES + (unsigned)(r & 1) * 8192u;
    u64 t0, t1;
    asm volatile("s_waitcnt vmcnt(0) lgkmcnt(0)\n\ts_memtime %0\n\ts_waitcnt lgkmcnt(0)" : "=s"(t0)::"memory");
    if (c.lane == 0) stw4(c.ws, o, 0, 0u);
    asm volatile("s_waitcnt vmcnt(0)\n\ts_memtime %0\n\ts_waitcnt lgkmcnt(0)" : "=s"(t1)::"memory");
    const unsigned d = (unsigned)(t1 - t0);
    if (r & 1) best[1] = d < best[1] ? d : best[1]; else best[0] = d < best[0] ? d : best[0];
  }
  if (c.lane == 0) set_prog(c, PHASE_OFF, best[1] < best[0] ? 1 : 0);
}

DEV void role_S(Ctx& c, const Params& p, int h, unsigned xcc) {
  constexpr unsigned XQ = 10240, XK = 12288;
  const unsigned ring = ring_off(p.ws);
  v4u w[2][20];
  stage_weights<10>(
      c, [&](int ch, int row, int c16) { return (row < 64 ? p.Wq : p.Wk) + (size_t)(64 * ch + (row & 63)) * 4096 + h * 128 + c16 * 8; },
      [&](int ch, unsigned img) {
#pragma unroll
        for (int t = 0; t < 2; ++t)
#pragma unroll
          for (int k2 = 0; k2 < 2; ++k2) w[t][2 * ch + k2] = wfrag(c.L, img, c.lane, t * 64 + 32 * k2, 32 * c.wave, 8);
      });
  stage_mem0(c, p);
  const unsigned phase = wait_phase(c);
  const int idx = (c.lane & 8) ? (int)0xEEEEEEEE : 0x44444444;
  const int g = c.lane >> 4, r16 = c.lane & 15, i8 = c.lane & 7, ih = (c.lane >> 3) & 1;
  const unsigned va = ring + remap(RMS_L + (unsigned)c.wave * 1280u + (unsigned)c.lane * 16u, phase);
  const unsigned vb = ring + remap(RMS_L + (unsigned)c.wave * 1280u + 1024u + (unsigned)r16 * 16u, phase);
  const unsigned pt_o = ring + remap(RP_L + (unsigned)h * 512u + (unsigned)((ih * 2 + (g & 1)) * 8 + i8) * 16u + (unsigned)(g >> 1) * 8u, phase);
  const unsigned pm_o = g < 2 ? ring + remap(RP_L + 16384u + (unsigned)h * 128u + (unsigned)(g * 8 + i8) * 8u + (unsigned)ih * 4u, phase) : 0xf0000000u;
  const unsigned tro = (unsigned)(8 * g + ((c.lane >> 2) & 3)) * 16u + (unsigned)(c.lane & 1) * 8u;
  const int tok = 8 * (r16 >> 3) + 4 * ((r16 >> 1) & 1) + 2 * ((r16 >> 2) & 1) + (r16 & 1);
  const unsigned kx_o = (unsigned)(g * 16 + tok) * 16u;
  if (c.wave == 0) STAMP(700, h);
  const unsigned rowo = (unsigned)(((c.lane >> 2) & 1) + 4 * ((c.lane >> 3) & 1) + (c.lane & 2)) * 16u + (unsigned)(c.lane & 1) * 8u;
  const unsigned o0 = XM + (unsigned)g * 256u + (unsigned)(g & 1) * 128u + rowo, o1 = o0 ^ 128u;
  const unsigned xw = (unsigned)(16 * c.wave + r16) * 16u + (unsigned)(g & 1) * 8u + (unsigned)(c.lane >> 5) * 4u;
  auto rd = [&](int b) { const v4u r = cat(ldtr(c.L, o0 + b * 1024u), ldtr(c.L, o1 + b * 1024u)); asm volatile("" ::: "memory"); return r; };
  auto put = [&](unsigned img, const f32x4& d) {
    const float y0 = swapadd32(d[0], d[2]), y1 = swapadd32(d[1], d[3]);
    *(LAS unsigned*)(c.L + img + xw) = pk2(y0, y1);
  };
  auto steps = [&](auto KIND) {
    constexpr int K = decltype(KIND)::value;
    int qk_seen = -1;
    if (K == 0) __builtin_amdgcn_s_setprio(2);
    for (int s = 0; s < NSEG; ++s) {
      v4u kx[8];
      if (K == 0) {
        wait_seen(c, PG_QK + (unsigned)h * 128u, 1, s, qk_seen);
#pragma unroll
        for (int f = 0; f < 8; ++f) kx[f] = ldw16(c.ws, kx_o, QK_OFF + (unsigned)(s * 32 + h) * 16384u + 8192u + f * 1024u);
        peek_use(peek_req(c, PG_QK + (unsigned)h * 128u, 1), qk_seen);
        asm volatile("" : "+v"(kx[0]), "+v"(kx[1]), "+v"(kx[2]), "+v"(kx[3]), "+v"(kx[4]), "+v"(kx[5]), "+v"(kx[6]), "+v"(kx[7]));
      }
      f32x4 dq = {0, 0, 0, 0}, dk = {0, 0, 0, 0};
      asm volatile("" : "+v"(dq), "+v"(dk));
      if (s > 0) {
        v4u a, b;
        poll2(c, va, vb, (unsigned)(s & 3) * RM_STR, a, b);
        sts16(c.L, XM + (unsigned)c.wave * 1280u + (unsigned)c.lane * 16u, a);
        sts16(c.L, XM + (unsigned)c.wave * 1280u + 1024u + (unsigned)r16 * 16u, b);
      }
      RTS(r0);
      wg_barrier();
      CYC(t1);
      constexpr int AH = K == 0 ? 4 : 7;
      v4u a[10], a2[10];
#pragma unroll
      for (int b = 0; b < AH; ++b) a[b] = rd(b);
#pragma unroll
      for (int b = 0; b < 10; ++b) {
        smf64_o(dq, a[b], cat8(w[0][2 * b], w[0][2 * b + 1]), idx);
        if (b + AH < 10) a[b + AH] = rd(b + AH);
        else if (K == 0) a2[b + AH - 10] = rd(b + AH - 10);
      }
      auto kstep = [&](int b) {
        smf64_o(dk, K == 0 ? a2[b] : a[b], cat8(w[1][2 * b], w[1][2 * b + 1]), idx);
        if (K == 0 && b + AH < 10) a2[b + AH] = rd(b + AH);
      };
      kstep(0); kstep(1); kstep(2);
      asm volatile("s_nop 4" : "+v"(dq));
      put(XQ, dq);
      kstep(3); kstep(4); kstep(5); kstep(6);
      wg_barrier();
      v4u qf[4];
      if (K == 0) {
#pragma unroll
        for (int ks = 0; ks < 4; ++ks) { qf[ks] = cat(ldtr(c.L, XQ + tro + ks * 512u), ldtr(c.L, XQ + tro + ks * 512u + 64u)); asm volatile("" ::: "memory"); }
      }
      kstep(7); kstep(8); kstep(9);
      if (K != 0) {
        asm volatile("s_nop 7" : "+v"(dk));
        put(XK, dk);
        CYC(t2);
        wg_barrier();
      }
      if (K == 0) {
        f32x4 sm, s0, s1;
        mfma_o0(s0, kx[0], qf[0]); mfma_o0(s1, kx[4], qf[0]);
        asm volatile("s_nop 5" : "+v"(dk));
        put(XK, dk);
        mfma_o(s0, kx[1], qf[1]); mfma_o(s1, kx[5], qf[1]);
        CYC(t2);
        wg_barrier();
        CYC(t3);
        v4u kf[4];
#pragma unroll
        for (int ks = 0; ks < 4; ++ks) { kf[ks] = cat(ldtr(c.L, XK + tro + ks * 512u), ldtr(c.L, XK + tro + ks * 512u + 64u)); asm volatile("" ::: "memory"); }
        mfma_o(s0, kx[2], qf[2]); mfma_o(s1, kx[6], qf[2]); mfma_o(s0, kx[3], qf[3]); mfma_o(s1, kx[7], qf[3]);
        mfma_o0(sm, kf[0], qf[0]); mfma_o(sm, kf[1], qf[1]); mfma_o(sm, kf[2], qf[2]); mfma_o(sm, kf[3], qf[3]);
        asm volatile("s_nop 3" : "+v"(s0), "+v"(s1));
        float t[6];
#pragma unroll
        for (int r = 0; r < 4; ++r) t[r] = ih ? s1[r] : s0[r];
        float mx = fmx(fmx(t[0], t[1]), t[2]);
        asm volatile("" : "+v"(sm), "+v"(mx), "+v"(t[3]));
        t[4] = ih ? sm[2] : sm[0]; t[5] = ih ? sm[3] : sm[1];
        mx = fmx(fmx(fmx(t[3], t[4]), t[5]), mx);
        mx = fmx(mx, dppf<0x128>(mx));
        mx = max_x16(mx); mx = max_x32(mx);
        constexpr float CS = 0.08838834764831845f * 1.4426950408889634f;
        const float mo = -mx * CS;
        float e[6];
#pragma unroll
        for (int r = 0; r < 6; ++r) e[r] = __builtin_amdgcn_exp2f(__builtin_fmaf(t[r], CS, mo));
        float sum = __builtin_fmaf(0.5f, e[4] + e[5], (e[0] + e[1]) + (e[2] + e[3]));
        sum += dppf<0x128>(sum);
        sum = add_x16(sum); sum = add_x32(sum);
        const float inv = __builtin_amdgcn_rcpf(sum);
        v2u pt = {nosent(pk2(e[0] * inv, e[1] * inv)), nosent(pk2(e[2] * inv, e[3] * inv))};
        const unsigned pm = nosent(pk2(e[4] * inv, e[5] * inv));
        const unsigned so = (unsigned)(s & 3) * RP_STR;
        stw8(c.ws, pt_o, so, pt);
        stw4(c.ws, pm_o, so, pm);
        SB();
        const unsigned sr = (unsigned)((s + 2) & 3) * RP_STR;
        stw8(c.ws, pt_o, sr, v2u{~0u, ~0u});
        stw4(c.ws, pm_o, sr, ~0u);
        CYC(t4);
        STAMP(640 + h, s);
        PUTV(1000 + h, s, t2 - t1); PUTV(1032 + h, s, t3 - t2); PUTV(1064 + h, s, t4 - t3);
      } else if (K == 2) {
        v4u kf[2];
#pragma unroll
        for (int k2 = 0; k2 < 2; ++k2) {
          const v4u f0 = cat(ldtr(c.L, XK + tro + k2 * 1024u), ldtr(c.L, XK + tro + k2 * 1024u + 64u));
          const v4u f1 = cat(ldtr(c.L, XK + tro + k2 * 1024u + 512u), ldtr(c.L, XK + tro + k2 * 1024u + 576u));
          kf[k2] = ih ? f1 : f0;
        }
#pragma unroll
        for (int k2 = 0; k2 < 2; ++k2)
          stw16(c.ws, (unsigned)(ih * 32 + g * 8 + i8) * 16u, KM_OFF + (unsigned)(s * 32 + h) * 2048u + k2 * 1024u, kf[k2]);
        drain_vm();
        if (c.lane == 0) set_prog(c, PG_KM + (unsigned)h * 128u, s);
      } else {
        __builtin_amdgcn_s_sleep(12);
      }
      PUTV(1100 + h * 8 + c.wave, s, r0);
    }
  };
  if (c.wave == 0) steps(IC<0>{}); else if (c.wave < 7) steps(IC<1>{}); else steps(IC<2>{});
}

DEV void role_C(Ctx& c, const Params& p, int ci) {
  constexpr unsigned ZL = 10240, RED = 16384;
  const unsigned ring = ring_off(p.ws);
  v4u w[2][20];
#pragma unroll
  for (int t = 0; t < 2; ++t)
#pragma unroll
    for (int kt = 0; kt < 20; ++kt)
      w[t][kt] = ldw16(c.ws, (unsigned)((c.lane >> 3) & 1) * 2048u + (unsigned)((c.lane >> 4) * 8 + (c.lane & 7)) * 16u,
                       WC_OFF + (unsigned)((2 * c.wave + t) * 5 + (kt >> 2)) * 327680u + (unsigned)ci * 4096u + (unsigned)(kt & 3) * 512u);
  stage_mem0(c, p);
  const unsigned phase = wait_phase(c);
  const int idx = (c.lane & 8) ? (int)0xEEEEEEEE : 0x44444444;
  const int g = c.lane >> 4, r16 = c.lane & 15, cc = c.lane & 7, hp = (c.lane >> 3) & 1;
  const unsigned va = ring + remap(RMC_L + (unsigned)c.wave * 1280u + (unsigned)c.lane * 16u, phase);
  const unsigned vb = ring + remap(RMC_L + (unsigned)c.wave * 1280u + 1024u + (unsigned)r16 * 16u, phase);
  const unsigned pa = ring + remap(RP_L + (unsigned)c.wave * 2048u + (unsigned)c.lane * 16u, phase);
  const unsigned pb = pa + 1024u;
  const unsigned pc = ring + remap(RP_L + 16384u + (unsigned)c.wave * 512u + (unsigned)c.lane * 8u, phase);
  const unsigned mpos = (unsigned)(ci ^ ((ci >> 1) & 1)) * 128u + (unsigned)(c.lane & 31) * 4u;
  const unsigned m_o = ring + remap((c.lane < 32 ? RMS_L : RMC_L) + mpos, phase);
  const int gbz = 2 * (c.lane >> 5) + hp;
  int vu_seen = -1;
  drain_vm();
  if (c.wave == 0) STAMP(701 + (ci >> 6), ci & 63);
  for (int s = 0; s < NSEG; ++s) {
    if (s > 0) {
      v4u a, b;
      poll2(c, va, vb, (unsigned)(s & 3) * RM_STR, a, b);
      sts16(c.L, XM + (unsigned)c.wave * 1280u + (unsigned)c.lane * 16u, a);
      sts16(c.L, XM + (unsigned)c.wave * 1280u + 1024u + (unsigned)r16 * 16u, b);
    }
    wg_barrier();
    if (s > 0 && c.tid == 64) set_prog(c, PG_Z + (unsigned)ci * 128u, s - 1);
    v4u u[4];
    wait_seen(c, PG_VU + (unsigned)(4 * c.wave) * 128u, 4, s, vu_seen);
#pragma unroll
    for (int j = 0; j < 4; ++j)
      u[j] = ldw16(c.ws, (unsigned)(g * 8 + cc) * 16u, UC_OFF + (unsigned)(s * 80 + ci) * 16384u + (unsigned)c.wave * 2048u + j * 512u);
    const int vu_peek = peek_req(c, PG_VU + (unsigned)(4 * c.wave) * 128u, 4);
    SB();
    f32x4 d0 = {0, 0, 0, 0}, d1 = {0, 0, 0, 0};
    asm volatile("s_nop 1" : "+v"(d0), "+v"(d1));
    project<5>(c, w, d0, d1, idx);
    mfma_done(d0, d1);
    v4u zf;
    v2u zo2;
    {
      const float z0 = swapadd32(d0[0], d1[0]), z1 = swapadd32(d0[1], d1[1]), z2 = swapadd32(d0[2], d1[2]), z3 = swapadd32(d0[3], d1[3]);
      zo2 = v2u{pk2(z0, z1), pk2(z2, z3)};
      const unsigned zo = ZL + (unsigned)c.wave * 512u + (unsigned)(gbz * 8 + cc) * 16u + (unsigned)(g & 1) * 4u;
      *(LAS unsigned*)(c.L + zo) = zo2.x;
      *(LAS unsigned*)(c.L + zo + 8) = zo2.y;
      wait_lds();
      zf = lds16(c.L, ZL + (unsigned)c.wave * 512u + (unsigned)(g * 8 + cc) * 16u);
    }
    if (s < NSEG - 1) {
      peek_use(vu_peek, vu_seen);
      f32x4 e0 = {0, 0, 0, 0}, e1 = {0, 0, 0, 0}, e2 = {0, 0, 0, 0};
      RTS(r1);
      poll3(c, pa, pb, pc, (unsigned)(s & 3) * RP_STR, cat8(u[0], u[1]), cat8(u[2], u[3]), zf, idx, e0, e1, e2);
      RTS(r2);
      e0 = (e0 + e1) + e2;
      const float y0 = swapadd32(e0[0], e0[2]), y1 = swapadd32(e0[1], e0[3]);
      sts8(c.L, RED + (unsigned)hp * 2048u + (unsigned)c.wave * 256u + (unsigned)cc * 32u + (unsigned)((g & 1) * 2 + (g >> 1)) * 8u,
           v2u{__float_as_uint(y0), __float_as_uint(y1)});
      wg_barrier();
      if (c.wave == 0) {
        f32x2 a8[8];
#pragma unroll
        for (int k = 0; k < 8; ++k) a8[k] = *(LAS f32x2*)(c.L + RED + (unsigned)(c.lane & 31) * 8u + k * 256u);
        const f32x2 t = ((a8[0] + a8[1]) + (a8[2] + a8[3])) + ((a8[4] + a8[5]) + (a8[6] + a8[7]));
        stw4(c.ws, m_o, (unsigned)((s + 1) & 3) * RM_STR, nosent(pk2(t[0], t[1])));
        SB();
        stw4(c.ws, m_o, (unsigned)((s + 3) & 3) * RM_STR, ~0u);
        STAMP(768 + ci, s);
      } else {
        __builtin_amdgcn_s_sleep(4);
      }
    }
    stw8(c.ws, (unsigned)(gbz * 8 + cc) * 16u + (unsigned)(g & 1) * 8u, ZC_OFF + (unsigned)(s * 80 + ci) * 4096u + (unsigned)c.wave * 512u, zo2);
  }
  drain_vm();
  wg_barrier();
  if (c.tid == 64) set_prog(c, PG_Z + (unsigned)ci * 128u, NSEG - 1);
}

DEV void role_QK(Ctx& c, const Params& p, int h) {
  const int STG_ID = h; (void)STG_ID;
  v4u w[2][20];
  const int wq = c.wave & 3, isk = c.wave >> 2;
  v4u xr[5];
  h_begin(c, p, 2 * h, xr);
  stage_weights<10>(
      c, [&](int ch, int row, int c16) { return (row < 64 ? p.Wq : p.Wk) + (size_t)(64 * ch + (row & 63)) * 4096 + h * 128 + c16 * 8; },
      [&](int ch, unsigned img) {
#pragma unroll
        for (int t = 0; t < 2; ++t)
#pragma unroll
          for (int k2 = 0; k2 < 2; ++k2) w[t][2 * ch + k2] = wfrag(c.L, img, c.lane, isk * 64 + 32 * k2, (32 * wq + 4 * t) * 2, 16);
        if (ch == 7) wait_h_all(c);
        if (ch == 8) {
#pragma unroll
          for (int m = 0; m < 5; ++m) dma_h(c, 0, m, H_IMG);
        }
        if (ch == 9) drain_vm();
      },
      [&] { h_finish(c, 2 * h, xr); });
  int km_seen = -1;
  if (c.wave == 0) STAMP(703, STG_ID);
  unsigned touch = 0;
  for (int t = 0; t < NSEG; ++t) {
    const unsigned aimg = (t & 1) ? 0u : H_IMG;
    const int tn = t + 1 < NSEG ? t + 1 : t;
    wait_seen(c, PG_KM + (unsigned)h * 128u, 1, t - AHEAD, km_seen);
    CYC(q0); RTS(qr0);
    const int km_peek = peek_req(c, PG_KM + (unsigned)h * 128u, 1);
    f32x4 acc[2][2];
#pragma unroll
    for (int a = 0; a < 2; ++a)
#pragma unroll
      for (int b = 0; b < 2; ++b) acc[a][b] = f32x4{0, 0, 0, 0};
    v4u af[3][2];
#pragma unroll
    for (int kt = 0; kt < 2; ++kt) { af[kt][0] = afrag(c, aimg, 0, kt); af[kt][1] = afrag(c, aimg, 1, kt); }
#pragma unroll
    for (int kt = 0; kt < 20; ++kt) {
      if (kt + 2 < 20) { af[(kt + 2) % 3][0] = afrag(c, aimg, 0, kt + 2); af[(kt + 2) % 3][1] = afrag(c, aimg, 1, kt + 2); }
      if ((kt & 1) && kt < 10) dma_h(c, tn, kt >> 1, H_IMG - aimg);
      SB();
      acc[0][0] = mfma(w[0][kt], af[kt % 3][0], acc[0][0]); acc[1][0] = mfma(w[1][kt], af[kt % 3][0], acc[1][0]);
      acc[0][1] = mfma(w[0][kt], af[kt % 3][1], acc[0][1]); acc[1][1] = mfma(w[1][kt], af[kt % 3][1], acc[1][1]);
      SB();
    }
    asm volatile("" ::"v"(touch));
    peek_use(km_peek, km_seen);
#pragma unroll
    for (int tau = 0; tau < 2; ++tau) {
      v4u o = {pk2(acc[0][tau][0], acc[0][tau][1]), pk2(acc[0][tau][2], acc[0][tau][3]), pk2(acc[1][tau][0], acc[1][tau][1]),
               pk2(acc[1][tau][2], acc[1][tau][3])};
      stw16(c.ws, (unsigned)c.lane * 16u, QK_OFF + (unsigned)(t * 32 + h) * 16384u + (unsigned)(isk * 8 + tau * 4 + wq) * 1024u, o);
    }
    CYC(q1);
    const bool now = t < 3 || t == NSEG - 1;
    if (now) drain_vm();
    {
      const int y = h >> 3, ph = (t ^ (y >> 1)) & 1, su = t - 2 - ph;
      touch = ldw4(c.ws, su >= 0 && c.wave < 5 ? (unsigned)c.lane * 128u : 0xf0000000u,
                   UC_OFF + (unsigned)((su < 0 ? 0 : su) * 80 + 40 * (y & 1)) * 16384u + (unsigned)(ph * 40 + (h & 7) * 5 + c.wave) * 8192u);
    }
    asm volatile("s_waitcnt vmcnt(3)" ::: "memory");
    wg_barrier();
    CYC(q2); RTS(qr2);
    if (c.wave == 3 && h < 8) { PUTV(560 + h, t, q1 - q0); PUTV(568 + h, t, q2 - q1); PUTV(576 + h, t, qr2 - qr0); }
    if (c.tid == 0) set_prog(c, PG_QK + (unsigned)h * 128u, now ? t : t - 1);
  }
}

DEV void role_AT(Ctx& c, const Params& p, int task) {
  const int h = task >> 1, par = task & 1;
  const int g = c.lane >> 4, r16 = c.lane & 15;
  const int tk = 8 * (r16 >> 2) + (r16 & 3);
  const unsigned ko = 8192u + (unsigned)(tk >> 4) * 4096u + (unsigned)(g * 16 + (tk & 15)) * 16u;
  int qk_seen = -1;
  for (int s = par; s < NSEG; s += 2) {
    v4u qf[2][4], kf[8];
    wait_seen(c, PG_QK + (unsigned)h * 128u, 1, s, qk_seen);
    const unsigned so = QK_OFF + (unsigned)(s * 32 + h) * 16384u;
#pragma unroll
    for (int ks = 0; ks < 4; ++ks) {
      qf[0][ks] = ldw16(c.ws, (unsigned)c.lane * 16u, so + ks * 1024u);
      qf[1][ks] = ldw16(c.ws, (unsigned)c.lane * 16u, so + 4096u + ks * 1024u);
      kf[ks] = ldw16(c.ws, ko, so + ks * 1024u);
      kf[4 + ks] = ldw16(c.ws, ko, so + 64u + ks * 1024u);
    }
    constexpr float SC = 0.08838834764831845f, L2E = 1.4426950408889634f;
    float v[2][12];
#pragma unroll
    for (int qt = 0; qt < 2; ++qt) {
      f32x4 s0 = {0, 0, 0, 0}, s1 = {0, 0, 0, 0};
#pragma unroll
      for (int ks = 0; ks < 4; ++ks) { s0 = mfma(kf[ks], qf[qt][ks], s0); s1 = mfma(kf[4 + ks], qf[qt][ks], s1); }
      const int qi = 16 * qt + r16;
#pragma unroll
      for (int r = 0; r < 4; ++r) {
        v[qt][r] = (8 * g + r <= qi) ? s0[r] * SC : -1e9f;
        v[qt][4 + r] = (8 * g + 4 + r <= qi) ? s1[r] * SC : -1e9f;
      }
    }
    SB();
    wait_prog(c, PG_KM + (unsigned)h * 128u, 1, s);
    v4u km[4];
#pragma unroll
    for (int ks = 0; ks < 4; ++ks) km[ks] = ldw16(c.ws, (unsigned)(g * 8 + (r16 & 7)) * 16u, KM_OFF + (unsigned)(s * 32 + h) * 2048u + ks * 512u);
#pragma unroll
    for (int qt = 0; qt < 2; ++qt) {
      f32x4 sm = {0, 0, 0, 0};
#pragma unroll
      for (int ks = 0; ks < 4; ++ks) sm = mfma(km[ks], qf[qt][ks], sm);
      float* x = v[qt];
#pragma unroll
      for (int r = 0; r < 4; ++r) x[8 + r] = sm[r] * SC;
      float mx = x[0];
#pragma unroll
      for (int i = 1; i < 12; ++i) mx = fmx(mx, x[i]);
      mx = max_x16(mx); mx = max_x32(mx);
      float st = 0.f, sk = 0.f;
#pragma unroll
      for (int i = 0; i < 12; ++i) { x[i] = __builtin_amdgcn_exp2f((x[i] - mx) * L2E); if (i < 8) st += x[i]; else sk += x[i]; }
      float sum = st + 0.5f * sk;
      sum = add_x16(sum); sum = add_x32(sum);
      const float inv = __builtin_amdgcn_rcpf(sum);
      v4u pt = {pk2(x[0] * inv, x[1] * inv), pk2(x[2] * inv, x[3] * inv), pk2(x[4] * inv, x[5] * inv), pk2(x[6] * inv, x[7] * inv)};
      v2u pm = {pk2(x[8] * inv, x[9] * inv), pk2(x[10] * inv, x[11] * inv)};
      stw16(c.ws, (unsigned)c.lane * 16u, PT_OFF + (unsigned)((s * 2 + qt) * 32 + h) * 1024u, pt);
      stw8(c.ws, g < 2 ? (unsigned)r16 * 16u + (unsigned)g * 8u : 0xf0000000u, PM_OFF + (unsigned)((s * 2 + qt) * 32 + h) * 256u, pm);
    }
    drain_vm();
    if (c.lane == 0) set_prog(c, PG_P + (unsigned)(par * 32 + h) * 128u, s);
  }
}

DEV void role_VU(Ctx& c, const Params& p, int h) {
  const int STG_ID = 32 + h; (void)STG_ID;
  constexpr unsigned VIMG = 40960;
  v4u wv[20], wo[5][4];
  v4u xr[5];
  h_begin(c, p, 2 * h + 1, xr);
  stage_weights<10>(
      c,
      [&](int ch, int row, int c16) {
        return ch < 5 ? p.Wv + (size_t)(128 * ch + row) * 4096 + h * 128 + c16 * 8 : p.Wo + (size_t)(h * 128 + row) * DM + (ch - 5) * 128 + c16 * 8;
      },
      [&](int ch, unsigned img) {
#pragma unroll
        for (int k4 = 0; k4 < 4; ++k4) {
          const v4u f = wfrag(c.L, img, c.lane, 32 * k4, 32 * c.wave, 8);
          if (ch < 5) wv[4 * ch + k4] = f; else wo[ch - 5][k4] = f;
        }
        if (ch == 7) wait_h_all(c);
        if (ch == 8) {
#pragma unroll
          for (int m = 0; m < 5; ++m) dma_h(c, 0, m, H_IMG);
        }
        if (ch == 9) drain_vm();
      },
      [&] { h_finish(c, 2 * h + 1, xr); });
  int km_seen = -1;
  if (c.wave == 0) STAMP(703, STG_ID);
  const int g = c.lane >> 4, r16 = c.lane & 15;
  const unsigned vro = VIMG + (unsigned)c.lane * 16u;
  const unsigned vwo = VIMG + (unsigned)((r16 >> 2) & 1) * 4096u + (unsigned)(c.wave >> 1) * 1024u +
                       (unsigned)((2 * (c.wave & 1) + (g >> 1)) * 16 + 4 * (r16 >> 3) + (r16 & 3)) * 16u + (unsigned)(g & 1) * 8u;
  const unsigned uo = (unsigned)(r16 >> 3) * 16384u + (unsigned)(g * 8 + (r16 & 7)) * 16u;
  for (int t = -1; t < NSEG; ++t) {
    const unsigned aimg = (t & 1) ? H_IMG : 0u, vb = (unsigned)(t & 1) * 8192u;
    const int tn = t + 2 < NSEG ? t + 2 : NSEG - 1;
    wait_seen(c, PG_KM + (unsigned)h * 128u, 1, t - AHEAD, km_seen);
    const unsigned uso = UC_OFF + (unsigned)((t < 0 ? 0 : t) * 80 + 2 * c.wave) * 16384u + (unsigned)h * 512u;
    const unsigned uvo = t < 0 ? 0xf0000000u : uo;
    const int km_peek = peek_req(c, PG_KM + (unsigned)h * 128u, 1);
    v4u vf[2][4], af[3][2];
#pragma unroll
    for (int kt = 0; kt < 2; ++kt) { af[kt][0] = afrag(c, aimg, 0, kt); af[kt][1] = afrag(c, aimg, 1, kt); }
    SB();
#pragma unroll
    for (int ks = 0; ks < 4; ++ks) {
#pragma unroll
      for (int tau = 0; tau < 2; ++tau) vf[tau][ks] = lds16(c.L, vro + vb + tau * 4096u + ks * 1024u);
      SB();
    }
    f32x4 a0 = {0, 0, 0, 0}, a1 = {0, 0, 0, 0}, d0, d1;
#pragma unroll
    for (int kt = 0; kt < 20; ++kt) {
      if (kt + 2 < 20) { af[(kt + 2) % 3][0] = afrag(c, aimg, 0, kt + 2); af[(kt + 2) % 3][1] = afrag(c, aimg, 1, kt + 2); }
      if (kt < 3) { dma_h(c, tn, 2 * kt, H_IMG - aimg); if (kt < 2) dma_h(c, tn, 2 * kt + 1, H_IMG - aimg); }
      SB();
      if ((kt & 3) == 0) { d0 = f32x4{0, 0, 0, 0}; d1 = f32x4{0, 0, 0, 0}; }
      if (kt == 0) { a0 = mfma(wv[kt], af[kt % 3][0], a0); a1 = mfma(wv[kt], af[kt % 3][1], a1); SB(); }
      d0 = mfma(vf[0][kt & 3], wo[kt >> 2][kt & 3], d0); d1 = mfma(vf[1][kt & 3], wo[kt >> 2][kt & 3], d1);
      SB();
      if (kt > 0) { a0 = mfma(wv[kt], af[kt % 3][0], a0); a1 = mfma(wv[kt], af[kt % 3][1], a1); SB(); }
      if ((kt & 3) == 3) {
        v4u o = {pk2(d0[0], d0[1]), pk2(d0[2], d0[3]), pk2(d1[0], d1[1]), pk2(d1[2], d1[3])};
        stw16(c.ws, uvo, uso + (unsigned)(kt >> 2) * 262144u, o);
      }
      SB();
    }
    peek_use(km_peek, km_seen);
    sts8(c.L, vwo + (8192u - vb), v2u{pk2(a0[0], a0[1]), pk2(a0[2], a0[3])});
    sts8(c.L, vwo + (8192u - vb) + 128u, v2u{pk2(a1[0], a1[1]), pk2(a1[2], a1[3])});
    const bool now = t < 3 || t == NSEG - 1;
    if (now) drain_vm(); else asm volatile("s_waitcnt vmcnt(5)" ::: "memory");
    wg_barrier();
    if (t >= 0 && c.tid == 0) set_prog(c, PG_VU + (unsigned)h * 128u, now ? t : t - 1);
  }
}

DEV void red_write(Ctx& c, unsigned R, const f32x4 (&d)[4]) {
  const int g = c.lane >> 4, tok = c.lane & 15;
#pragma unroll
  for (int ct = 0; ct < 4; ++ct)
    sts16(c.L, R + (unsigned)c.wave * 4096u + (unsigned)tok * 256u + (unsigned)(((ct * 4 + g) ^ (tok & 7)) * 16), __builtin_bit_cast(v4u, d[ct]));
}
DEV f32x4 red_read(Ctx& c, unsigned R, int v) {
  const int tok = 4 * v + (c.lane >> 4), qd = c.lane & 15;
  const unsigned o = R + (unsigned)tok * 256u + (unsigned)((qd ^ (tok & 7)) * 16);
  f32x4 t = __builtin_bit_cast(f32x4, lds16(c.L, o));
#pragma unroll
  for (int w = 1; w < 8; ++w) t += __builtin_bit_cast(f32x4, lds16(c.L, o + w * 4096u));
  return t;
}

template <bool STORER>
DEV void role_OUT(Ctx& c, const Params& p, int o) {
  const int par = o / 10, cbk = o - 10 * par;
  const int g = c.lane >> 4, r16 = c.lane & 15;
  const float* mod = (const float*)(p.ws + MOD_OFF);
  const f32x4 ga = *(const f32x4*)(mod + 2 * DM + 64 * cbk + 4 * r16);
  const unsigned uo = (unsigned)(r16 >> 3) * 16384u + (unsigned)(g * 8 + (r16 & 7)) * 16u;
  const unsigned zo = (unsigned)(r16 >> 3) * 4096u + (unsigned)(g * 8 + (r16 & 7)) * 16u;
  const int rj = (cbk % 5) >> 1, rq = (cbk % 5) & 1;
  int vu_seen = -1;
  v4u u[4][4], un[4][4];
  auto fetch = [&](int s) {
#pragma unroll
    for (int ct = 0; ct < 4; ++ct)
#pragma unroll
      for (int j = 0; j < 4; ++j)
        un[ct][j] = ldw16(c.ws, uo, UC_OFF + (unsigned)(s * 80 + 8 * cbk + 2 * ct) * 16384u + (unsigned)c.wave * 2048u + (unsigned)((j + 2 * rj) & 3) * 512u);
  };
  wait_seen(c, PG_VU + (unsigned)(4 * c.wave) * 128u, 4, par, vu_seen);
  fetch(par);
  for (int s = par; s < NSEG; s += 2) {
    const int sn = s + 2 < NSEG ? s + 2 : s;
    v4u z[4], pf[2][5];
    v2u xv[4];
    if (STORER) {
#pragma unroll
      for (int v = 0; v < 4; ++v) xv[v] = *(const v2u*)(p.x + (size_t)(s * 32 + 16 * c.wave + 4 * v + g) * DM + 64 * cbk + 4 * r16);
    }
    wait_seen(c, PG_VU + (unsigned)(4 * c.wave) * 128u, 4, sn, vu_seen);
    wait_prog(c, PG_Z + (unsigned)(8 * cbk) * 128u, 8, s);
#pragma unroll
    for (int ct = 0; ct < 4; ++ct) z[ct] = ldw16(c.ws, zo, ZC_OFF + (unsigned)(s * 80 + 8 * cbk + 2 * ct) * 4096u + (unsigned)c.wave * 512u);
    wait_prog(c, PG_P + (unsigned)(par * 32 + 4 * c.wave) * 128u, 4, s);
#pragma unroll
    for (int ct = 0; ct < 4; ++ct)
#pragma unroll
      for (int j = 0; j < 4; ++j) u[ct][j] = un[ct][j];
#pragma unroll
    for (int qt = 0; qt < 2; ++qt) {
#pragma unroll
      for (int j = 0; j < 4; ++j)
        pf[qt][j] = ldw16(c.ws, (unsigned)c.lane * 16u, PT_OFF + (unsigned)((s * 2 + (qt ^ rq)) * 32 + 4 * c.wave + ((j + 2 * rj) & 3)) * 1024u);
      pf[qt][4] = ldw16(c.ws, (unsigned)c.lane * 16u, PM_OFF + (unsigned)((s * 2 + (qt ^ rq)) * 8 + c.wave) * 1024u);
    }
    SB();
#pragma unroll
    for (int qt = 0; qt < 2; ++qt) {
      f32x4 d[4];
#pragma unroll
      for (int ct = 0; ct < 4; ++ct) {
        d[ct] = f32x4{0, 0, 0, 0};
#pragma unroll
        for (int j = 0; j < 4; ++j) d[ct] = mfma(u[ct][j], pf[qt][j], d[ct]);
        d[ct] = mfma(z[ct], pf[qt][4], d[ct]);
      }
      red_write(c, (unsigned)(qt ^ rq) * 32768u, d);
    }
    wg_barrier();
    if (!STORER) fetch(sn);
    if (STORER) {
#pragma unroll
      for (int v = 0; v < 4; ++v) {
        const f32x4 t = red_read(c, (unsigned)c.wave * 32768u, v);
        v2u ov = {pk2(bflo(xv[v].x) + rbf(ga[0] * rbf(t[0])), bfhi(xv[v].x) + rbf(ga[1] * rbf(t[1]))),
                  pk2(bflo(xv[v].y) + rbf(ga[2] * rbf(t[2])), bfhi(xv[v].y) + rbf(ga[3] * rbf(t[3])))};
        stw8(c.ws, (unsigned)(16 * c.wave + 4 * v + g) * 1280u + (unsigned)(64 * cbk + 4 * r16) * 2u, X1_OFF + (unsigned)s * 40960u, ov);
      }
      drain_vm();
    }
    wg_barrier();
    if (c.tid == 0) set_prog(c, PG_X1 + (unsigned)o * 128u, s);
    if (STORER) fetch(sn);
  }
}

DEV void role_F1(Ctx& c, const Params& p, int o) {
  constexpr unsigned AIMG = 0, MODL = 40960;
  const int par = o / 14, fi = o - 14 * par;
  v4u wg[20], wu[20];
  stage_weights<10>(
      c,
      [&](int ch, int row, int c16) {
        const int col = 128 * fi + c16 * 8;
        return (ch < 5 ? p.w1 + (size_t)(128 * ch + row) * FF : p.w3 + (size_t)(128 * (ch - 5) + row) * FF) + (col < FF ? col : 0);
      },
      [&](int ch, unsigned img) {
#pragma unroll
        for (int k4 = 0; k4 < 4; ++k4) {
          const v4u f = wfrag(c.L, img, c.lane, 32 * k4, 32 * c.wave, 8);
          if (ch < 5) wg[4 * ch + k4] = f; else wu[4 * (ch - 5) + k4] = f;
        }
      });
  load_modl(c, p, MODL, 3, 4);
  wg_barrier();
  const int g = c.lane >> 4, r16 = c.lane & 15;
  const int tp = 8 * fi + c.wave;
  const unsigned ho = (unsigned)(tp >> 1) * 1024u + (unsigned)((2 * (tp & 1) + (g >> 1)) * 16 + r16) * 16u + (unsigned)(g & 1) * 8u;
  for (int s = par; s < NSEG; s += 2) {
    v4u xr[5];
    wait_prog(c, PG_X1 + (unsigned)(par * 10) * 128u, 10, s);
#pragma unroll
    for (int m = 0; m < 5; ++m)
      xr[m] = ldw16(c.ws, (unsigned)(4 * c.wave + g) * 1280u + (unsigned)r16 * 16u + m * 256u, X1_OFF + (unsigned)s * 40960u);
    ln_rows(c, xr, AIMG, MODL);
    wg_barrier();
    if (tp < 108) {
#pragma unroll
      for (int tau = 0; tau < 2; ++tau) {
        f32x4 a = {0, 0, 0, 0}, b = {0, 0, 0, 0};
        v4u af[5];
#pragma unroll
        for (int kt = 0; kt < 4; ++kt) af[kt] = afrag(c, AIMG, tau, kt);
#pragma unroll
        for (int kt = 0; kt < 20; ++kt) {
          if (kt + 4 < 20) af[(kt + 4) % 5] = afrag(c, AIMG, tau, kt + 4);
          SB();
          a = mfma(wg[kt], af[kt % 5], a); b = mfma(wu[kt], af[kt % 5], b);
          SB();
        }
        float y[4];
#pragma unroll
        for (int r = 0; r < 4; ++r) {
          const float gt = rbf(a[r]);
          const float sl = rbf(gt * __builtin_amdgcn_rcpf(1.f + __builtin_amdgcn_exp2f(-1.4426950408889634f * gt)));
          y[r] = sl * rbf(b[r]);
        }
        stw8(c.ws, ho, HF_OFF + (unsigned)(s * 2 + tau) * 55296u, v2u{pk2(y[0], y[1]), pk2(y[2], y[3])});
      }
      drain_vm();
    }
    wg_barrier();
    if (c.tid == 0) set_prog(c, PG_HF + (unsigned)o * 128u, s);
  }
}

DEV void role_F2(Ctx& c, const Params& p, int o) {
  const int tau = o & 1, cbk = o >> 1;
  const int g = c.lane >> 4, r16 = c.lane & 15;
  const int k0 = 7 * c.wave, rot = 2 * (cbk & 3);
  auto kstep = [&](int j) { const int r = j + rot; return k0 + (r >= 7 ? r - 7 : r); };
  v4u w[7][4];
  {
    const unsigned img = (unsigned)c.wave * 5120u;
    v4u st[7][4];
#pragma unroll
    for (int j = 0; j < 7; ++j) {
      const int kt = kstep(j) < 54 ? kstep(j) : 53;
#pragma unroll
      for (int m = 0; m < 4; ++m) st[j][m] = *(const v4u*)(p.w2 + (size_t)(32 * kt + (c.lane >> 3) + 8 * m) * DM + 64 * cbk + (c.lane & 7) * 8);
    }
#pragma unroll
    for (int j = 0; j < 7; ++j) {
#pragma unroll
      for (int m = 0; m < 4; ++m) sts16(c.L, img + (unsigned)((c.lane >> 3) + 8 * m) * 160u + (unsigned)(c.lane & 7) * 16u, st[j][m]);
      wait_lds();
#pragma unroll
      for (int ct = 0; ct < 4; ++ct) {
        const unsigned a = img + (unsigned)(8 * g + ((c.lane >> 2) & 3)) * 160u + (unsigned)(32 * ct + (c.lane & 3) * 8);
        w[j][ct] = cat(ldtr(c.L, a), ldtr(c.L, a + 4 * 160));
        if (kstep(j) >= 54) w[j][ct] = v4u{0, 0, 0, 0};
      }
      wait_lds();
    }
  }
  wg_barrier();
  const float* mod = (const float*)(p.ws + MOD_OFF);
  const f32x4 gb = *(const f32x4*)(mod + 5 * DM + 64 * cbk + 4 * r16);
  const int f0 = (14 * c.wave) >> 3, nf = (c.wave == 7 ? 13 : (14 * c.wave + 13) >> 3) - f0 + 1;
  bool next_ok = false;
  for (int s = 0; s < NSEG; ++s) {
    v4u hf[7];
    if (!next_ok) wait_prog(c, PG_HF + (unsigned)((s & 1) * 14 + f0) * 128u, nf, s);
#pragma unroll
    for (int j = 0; j < 7; ++j) {
      const int kt = kstep(j) < 54 ? kstep(j) : 53;
      hf[j] = ldw16(c.ws, (unsigned)c.lane * 16u, HF_OFF + (unsigned)((s * 2 + tau) * 54 + kt) * 1024u);
    }
    const int row = s * 32 + 16 * tau + 4 * (c.wave & 3) + g;
    const v2u xv = ldw8(c.ws, (unsigned)row * 1280u + (unsigned)(64 * cbk + 4 * r16) * 2u, X1_OFF);
    const int pk = peek_req(c, PG_HF + (unsigned)(((s + 1) & 1) * 14 + f0) * 128u, nf);
    SB();
    f32x4 d[4];
#pragma unroll
    for (int ct = 0; ct < 4; ++ct) {
      d[ct] = f32x4{0, 0, 0, 0};
#pragma unroll
      for (int j = 0; j < 7; ++j) d[ct] = mfma(w[j][ct], hf[j], d[ct]);
    }
    next_ok = __all(pk >= s + 1);
    const unsigned R = (unsigned)(s & 1) * 32768u;
    red_write(c, R, d);
    wg_barrier();
    if (c.wave < 4) {
      const f32x4 t = red_read(c, R, c.wave);
      v2u ov = {pk2(bflo(xv.x) + rbf(gb[0] * rbf(t[0])), bfhi(xv.x) + rbf(gb[1] * rbf(t[1]))),
                pk2(bflo(xv.y) + rbf(gb[2] * rbf(t[2])), bfhi(xv.y) + rbf(gb[3] * rbf(t[3])))};
      *(v2u*)(p.out + (size_t)row * DM + 64 * cbk + 4 * r16) = ov;
      if (c.wave == 0) STAMP(900 + o, s);
    }
  }
}

DEV void dispatch(Ctx& c, const Params& p, int x, int t, unsigned xcc) {
  if (x == 0) { role_S(c, p, t, xcc); return; }
  const int y = x - 4;
  if (x >= 4 && t < 8) { role_QK(c, p, y * 8 + t); return; }
  if (x >= 4 && t < 16) { role_VU(c, p, y * 8 + t - 8); return; }
  if (x < 4) {
    const int nc = x == 3 ? 26 : 27;
    if (t < nc) { role_C(c, p, (x - 1) * 27 + t); return; }
    const int e = (x - 1) * 5 + (t - nc);
    if (e < 11) { if (c.wave < 6 && e * 6 + c.wave < 64) role_AT(c, p, e * 6 + c.wave); return; }
    if (e == 15) { if (c.wave == 0) pick_phase(c, p); return; }
    wait_prog(c, PG_KM, 1, 0);
    role_F2(c, p, 16 + (e - 11));
    return;
  }
  if (t >= 23 && t < 28) { if (c.wave < 2) role_OUT<true>(c, p, y * 5 + t - 23); else role_OUT<false>(c, p, y * 5 + t - 23); return; }
  wait_prog(c, PG_QK + (unsigned)(y * 8) * 128u, 8, 0);
  if (t < 23) { role_F1(c, p, y * 7 + t - 16); return; }
  role_F2(c, p, (y >> 1) + 2 * ((y & 1) * 4 + t - 28));
}

__global__ void __launch_bounds__(512, 2) k_main(Params p) {
  extern __shared__ __attribute__((aligned(16))) char lds[];
  Ctx c;
  c.L = (LAS char*)lds;
  c.tid = threadIdx.x; c.lane = c.tid & 63; c.wave = __builtin_amdgcn_readfirstlane(c.tid >> 6);
  c.ws = __builtin_amdgcn_make_buffer_rsrc(p.ws, 0, WS_END, 0x00020000);
  c.srd = v4u{(unsigned)(u64)p.ws, (unsigned)((u64)p.ws >> 32) & 0xffffu, WS_END, 0x00020000u};
  c.budget = SPIN_BUDGET;
  const unsigned xcc = (unsigned)__builtin_amdgcn_s_getreg(20 | (3 << 11)) & 7u;
  if (c.wave == 0) {
    int role = 0;
    int t = 0;
    if (c.lane == 0) t = (int)__hip_atomic_fetch_add((unsigned*)(p.ws + TICK_OFF + xcc * 128u), 1u, __ATOMIC_RELAXED, __HIP_MEMORY_SCOPE_AGENT) + 1;
    t = __builtin_amdgcn_readfirstlane(t);
    if (t < 32) {
      role = (int)xcc * 32 + t;
    } else {
      int n[8], tot;
      for (;;) {
        asm volatile("" ::: "memory");
        const int v = (int)ldw4(c.ws, TICK_OFF + (unsigned)(c.lane & 7) * 128u, 0) + 1;
        tot = 0;
#pragma unroll
        for (int x = 0; x < 8; ++x) { n[x] = __builtin_amdgcn_readlane(v, x); tot += n[x]; }
        if (tot >= 256 || c.budget == 0) break;
        --c.budget;
        __builtin_amdgcn_s_sleep(8);
      }
      int rank = t - 32;
#pragma unroll
      for (int x = 0; x < 8; ++x) if (x < (int)xcc && n[x] > 32) rank += n[x] - 32;
      role = -1;
#pragma unroll
      for (int x = 0; x < 8; ++x) {
        const int fr = n[x] < 32 ? 32 - n[x] : 0;
        if (role < 0 && rank < fr) role = x * 32 + n[x] + rank;
        if (role < 0) rank -= fr;
      }
    }
    if (c.lane == 0) *(LAS int*)(c.L + LDS_BYTES - 16) = role;
  }
  wg_barrier();
  const int role = __builtin_amdgcn_readfirstlane(*(LAS int*)(c.L + LDS_BYTES - 16));
  if (role < 0) return;
  if (c.wave == 0) STAMP(600 + (role >> 6), role & 63);
  const int x = role >> 5, t = role & 31;
  CYC(k0); RTS(kr0);
  dispatch(c, p, x, t, xcc);
  CYC(k1); RTS(kr1);
  if (c.wave == 0) { PUTV(610 + (role >> 6), role & 63, k1 - k0); PUTV(614 + (role >> 6), role & 63, kr1 - kr0); }
}

constexpr int INIT_FILL_BLOCKS = (CTRL_BYTES + RING_BYTES) / 16384;
static_assert((CTRL_BYTES + RING_BYTES) % 16384 == 0 && 220 + INIT_FILL_BLOCKS <= 256, "init grid");
__global__ void __launch_bounds__(512, 2) k_init(Params p) {
  extern __shared__ __attribute__((aligned(16))) char lds[];
  const int tid = threadIdx.x, blk = blockIdx.x;
  const rsrc_t ws = __builtin_amdgcn_make_buffer_rsrc(p.ws, 0, WS_END, 0x00020000);
  if (blk < 160) {
    Ctx c;
    c.L = (LAS char*)lds; c.tid = tid; c.lane = tid & 63; c.wave = __builtin_amdgcn_readfirstlane(tid >> 6); c.ws = ws;
    b_item(c, p, (blk & 7) * 20 + (blk >> 3));
  } else if (blk < 220) {
    float* red = (float*)lds;
    const int kg = tid >> 3, cq = tid & 7, cb = blk - 160;
    v4u wv[10];
#pragma unroll
    for (int i = 0; i < 10; ++i) wv[i] = *(const v4u*)(p.wmod + (size_t)(kg + 64 * i) * 3840 + cb * 64 + cq * 8);
    float acc[8] = {0, 0, 0, 0, 0, 0, 0, 0};
#pragma unroll
    for (int i = 0; i < 10; ++i) {
      const float cv = bflo(p.c[kg + 64 * i]);
      const float sv = rbf(cv / (1.f + __expf(-cv)));
#pragma unroll
      for (int d = 0; d < 4; ++d) { const unsigned u = wv[i][d]; acc[2 * d] += sv * bflo(u); acc[2 * d + 1] += sv * bfhi(u); }
    }
#pragma unroll
    for (int j = 0; j < 8; ++j) red[kg * 65 + cq * 8 + j] = acc[j];
    __syncthreads();
    if (tid < 64) {
      float s = 0.f;
#pragma unroll
      for (int k = 0; k < 64; ++k) s += red[k * 65 + tid];
      ((float*)(p.ws + MOD_OFF))[cb * 64 + tid] = rbf(s);
    }
  } else {
#pragma unroll
    for (int m = 0; m < 2; ++m) {
      const unsigned o = (unsigned)((blk - 220) * 1024 + m * 512 + tid) * 16u;
      stw16(ws, o < CTRL_BYTES ? o : ring_off(p.ws) + (o - CTRL_BYTES), 0, v4u{~0u, ~0u, ~0u, ~0u});
    }
  }
}

}

extern "C" void kernel_launch(void* const* d_in, const int* in_sizes, int n_in, void* d_out, int out_size, void* d_ws, size_t ws_size,
                              hipStream_t stream) {
  Params p;
  p.x = (const u16*)d_in[0]; p.mem0 = (const u16*)d_in[1]; p.Wq = (const u16*)d_in[2]; p.Wk = (const u16*)d_in[3];
  p.Wv = (const u16*)d_in[4]; p.Wo = (const u16*)d_in[5]; p.w1 = (const u16*)d_in[8]; p.w3 = (const u16*)d_in[9];
  p.w2 = (const u16*)d_in[10]; p.c = (const u16*)d_in[11]; p.wmod = (const u16*)d_in[12];
  p.out = (u16*)d_out; p.ws = (char*)d_ws;
  (void)hipFuncSetAttribute((const void*)k_main, hipFuncAttributeMaxDynamicSharedMemorySize, LDS_BYTES);
  (void)hipFuncSetAttribute((const void*)k_init, hipFuncAttributeMaxDynamicSharedMemorySize, 2 * WIMG);
  k_init<<<220 + INIT_FILL_BLOCKS, 512, 2 * WIMG, stream>>>(p);
  k_main<<<256, 512, LDS_BYTES, stream>>>(p);
}
```
